```python
import torch

def _softmax(x, axis=-1):
    return torch.softmax(x, dim=axis)
def _gelu(x):
    return torch.nn.functional.gelu(x, approximate="tanh")
def _silu(x):
    return torch.nn.functional.silu(x)
def _sigmoid(x):
    return torch.sigmoid(x)
def _layernorm(x, axis=-1, eps=1e-5):
    m = x.mean(dim=axis, keepdim=True)
    v = x.var(dim=axis, keepdim=True, unbiased=False)
    return (x - m) / torch.sqrt(v + eps)
def _scatter_add(tgt, idx, src, axis=0):
    return tgt.clone().index_add_(axis, idx, src)
def _segment_sum(src, seg, n):
    out = torch.zeros((n,) + src.shape[1:], dtype=src.dtype)
    return out.index_add_(0, seg, src)
def _pad(x, pw):
    flat = tuple(v for pair in reversed(pw) for v in pair)
    return torch.nn.functional.pad(x, flat)

kernel_name = 'standalone__kv_cache_quant_int4_int2_kv_with_per_channel_or_per_token_scales__decode'

def setup_inputs():

    _w = (lambda a: (lambda t: t.to(torch.bfloat16) if t.is_floating_point() else t)(torch.as_tensor(a)))
    B = 1
    Sq = 1
    Skv = 32768
    Hq = 32
    Hkv = 8
    Dh = 128
    bits = 4
    G = -1
    if G <= 0 or G >= Skv:
        G = max(1, Skv // 2)
    Skv = (Skv // G) * G
    ngk = Skv // G
    qmax = (1 << bits) - 1
    return {
        "q": _w((lambda *s: torch.randn(*s, dtype=torch.bfloat16) * 1.6)(B, Hq, Sq, Dh)),
        "qk": _w(torch.randint(0, qmax + 1, (B, Hkv, Skv, Dh)).to(torch.int32)),
        "qv": _w(torch.randint(0, qmax + 1, (B, Hkv, Skv, Dh)).to(torch.int32)),
        "k_scales": _w(torch.abs((lambda *s: torch.randn(*s, dtype=torch.bfloat16) * 1.6)(B, Hkv, ngk, Dh)) * 0.05 + 0.02),
        "k_zeros": _w((lambda *s: torch.randn(*s, dtype=torch.bfloat16) * 1.6)(B, Hkv, ngk, Dh) + float(qmax) / 2.0),
        "v_scales": _w(torch.abs((lambda *s: torch.randn(*s, dtype=torch.bfloat16) * 0.05)(B, Hkv, Skv, 1)) * 0.05 + 0.02),
        "v_zeros": _w((lambda *s: torch.randn(*s, dtype=torch.bfloat16) * 0.05)(B, Hkv, Skv, 1) + float(qmax) / 2.0),
    }

def reference(q, qk, qv, k_scales, k_zeros, v_scales, v_zeros):
    B, Hq, Sq, Dh = q.shape
    Hkv, Skv = qk.shape[1], qk.shape[2]
    ngk = k_scales.shape[2]
    G = Skv // ngk
    ks = (lambda x, n, axis: torch.repeat_interleave(x, n, dim=axis))(k_scales, G, axis=2)
    kz = (lambda x, n, axis: torch.repeat_interleave(x, n, dim=axis))(k_zeros, G, axis=2)
    k = ((lambda x, dt: x.to(dt))(qk, q.dtype) - kz) * ks
    v = ((lambda x, dt: x.to(dt))(qv, q.dtype) - v_zeros) * v_scales
    rep = Hq // Hkv
    if rep > 1:
        k = (lambda x, n, axis: torch.repeat_interleave(x, n, dim=axis))(k, rep, axis=1)
        v = (lambda x, n, axis: torch.repeat_interleave(x, n, dim=axis))(v, rep, axis=1)
    scale = float(Dh) ** -0.5
    logits = torch.einsum("bhqd,bhkd->bhqk", q, k) * scale
    p = _softmax(logits, axis=-1)
    o = torch.einsum("bhqk,bhkd->bhqd", p, v)
    return o
```

```cpp
#include <hip/hip_runtime.h>
#include <hip/hip_bf16.h>
#include <stdint.h>

#define KV_NT 1
#define WAVES 8
#define NSPLIT 32

namespace {

constexpr int HQ = 32, HKV = 8, GQ = HQ / HKV, SKV = 32768, D = 128, NGK = 2, GTOK = SKV / NGK;
constexpr int NTHR = WAVES * 64;
constexpr int TOK_WG = SKV / NSPLIT;
constexpr int TOK_WAVE = TOK_WG / WAVES;
constexpr int TILE = 32;
constexpr int NTILE = TOK_WAVE / TILE;
static_assert(GQ == 4 && TOK_WAVE % TILE == 0 && GTOK % TOK_WAVE == 0, "shape");

typedef int    i32x4 __attribute__((ext_vector_type(4)));
typedef unsigned u32x4 __attribute__((ext_vector_type(4)));
typedef unsigned u32x2 __attribute__((ext_vector_type(2)));
typedef float  f32x4 __attribute__((ext_vector_type(4)));
typedef float  f32x2 __attribute__((ext_vector_type(2)));
typedef __bf16 bf16x8 __attribute__((ext_vector_type(8)));
typedef __bf16 bf16x2 __attribute__((ext_vector_type(2)));

__device__ __forceinline__ i32x4 ldkv(const int* p) {
  return __builtin_nontemporal_load(reinterpret_cast<const i32x4*>(p));
}
__device__ __forceinline__ unsigned pk_bf16(float lo, float hi) {
  f32x2 f = {lo, hi};
  bf16x2 b = __builtin_convertvector(f, bf16x2);
  return __builtin_bit_cast(unsigned, b);
}
__device__ __forceinline__ float bf_lo(unsigned u) { return __builtin_bit_cast(float, u << 16); }
__device__ __forceinline__ float bf_hi(unsigned u) { return __builtin_bit_cast(float, u & 0xffff0000u); }
template <int CTRL> __device__ __forceinline__ float dpp(float x) {
  return __builtin_bit_cast(float, __builtin_amdgcn_mov_dpp(__builtin_bit_cast(int, x), CTRL, 0xf, 0xf, true));
}
constexpr int XOR1 = 0xB1, XOR2 = 0x4E, ROR4 = 0x124, ROR8 = 0x128;
__device__ __forceinline__ float xrow_max(float x) {
  auto s = __builtin_amdgcn_permlane16_swap(__float_as_uint(x), __float_as_uint(x), false, false);
  x = fmaxf(__uint_as_float(s[0]), __uint_as_float(s[1]));
  auto t = __builtin_amdgcn_permlane32_swap(__float_as_uint(x), __float_as_uint(x), false, false);
  return fmaxf(__uint_as_float(t[0]), __uint_as_float(t[1]));
}
__device__ __forceinline__ float xrow_sum(float x) {
  auto s = __builtin_amdgcn_permlane16_swap(__float_as_uint(x), __float_as_uint(x), false, false);
  x = __uint_as_float(s[0]) + __uint_as_float(s[1]);
  auto t = __builtin_amdgcn_permlane32_swap(__float_as_uint(x), __float_as_uint(x), false, false);
  return __uint_as_float(t[0]) + __uint_as_float(t[1]);
}
__device__ __forceinline__ f32x4 mfma16(u32x4 a, u32x4 b, f32x4 c) {
  return __builtin_amdgcn_mfma_f32_16x16x32_bf16(__builtin_bit_cast(bf16x8, a), __builtin_bit_cast(bf16x8, b), c, 0, 0, 0);
}
#define SB() __builtin_amdgcn_sched_barrier(0)

constexpr int REC = 16 + GQ * D;

__global__ void __launch_bounds__(NTHR, 2) attn_split(const unsigned short* __restrict__ q, const int* __restrict__ qk,
                                                      const int* __restrict__ qv, const unsigned short* __restrict__ ks,
                                                      const unsigned short* __restrict__ kz, const unsigned short* __restrict__ vs,
                                                      const unsigned short* __restrict__ vz, float* __restrict__ po,
                                                      float* __restrict__ pml) {
  __shared__ __attribute__((aligned(16))) float lds[WAVES * REC];
  const int tid = threadIdx.x, lane = tid & 63;
  const int wave = __builtin_amdgcn_readfirstlane(tid >> 6);
  const int bid = blockIdx.x;
  const int hk = bid / NSPLIT, sp = bid % NSPLIT;
  const int tok0 = sp * TOK_WG + wave * TOK_WAVE;
  const int grp = tok0 / GTOK;
  const int g = lane >> 4;

  const int hq = (lane >> 2) & 3, cq = lane & 3;
  const int dq = 16 * g + 4 * cq;
  u32x2 qr[2], ksr[2], kzr[2];
#pragma unroll
  for (int h = 0; h < 2; ++h) {
    qr[h]  = *reinterpret_cast<const u32x2*>(q + (hk * GQ + hq) * D + 64 * h + dq);
    ksr[h] = *reinterpret_cast<const u32x2*>(ks + (hk * NGK + grp) * D + 64 * h + dq);
    kzr[h] = *reinterpret_cast<const u32x2*>(kz + (hk * NGK + grp) * D + 64 * h + dq);
  }
  const unsigned short* vsp = vs + (size_t)hk * SKV + tok0 + 8 * g;
  const unsigned short* vzp = vz + (size_t)hk * SKV + tok0 + 8 * g;
  u32x4 vsn = *reinterpret_cast<const u32x4*>(vsp);
  u32x4 vzn = *reinterpret_cast<const u32x4*>(vzp);
  SB();
  const int* kp = qk + ((size_t)hk * SKV + tok0 + 8 * hq) * D + dq;
  const int* vp = qv + ((size_t)hk * SKV + tok0 + 8 * g) * D + 4 * (lane & 15);
  i32x4 K[16], V[16];
#pragma unroll
  for (int j = 0; j < 8; ++j) { K[2 * j] = ldkv(kp + j * D); K[2 * j + 1] = ldkv(kp + j * D + 64); }
  SB();
#pragma unroll
  for (int j = 0; j < 8; ++j) { V[2 * j] = ldkv(vp + j * D); V[2 * j + 1] = ldkv(vp + j * D + 64); }
  SB();

  constexpr float C2 = 0.088388347648318440f * 1.4426950408889634f;
  u32x4 Qh, Ql; float bias = 0.f;
#pragma unroll
  for (int h = 0; h < 2; ++h)
#pragma unroll
    for (int w = 0; w < 2; ++w) {
      const float a0 = bf_lo(qr[h][w]) * bf_lo(ksr[h][w]) * C2, a1 = bf_hi(qr[h][w]) * bf_hi(ksr[h][w]) * C2;
      const unsigned hi = pk_bf16(a0, a1);
      const unsigned lo = pk_bf16(a0 - bf_lo(hi), a1 - bf_hi(hi));
      Qh[2 * h + w] = hi; Ql[2 * h + w] = lo;
      bias = fmaf(-(bf_lo(hi) + bf_lo(lo)), bf_lo(kzr[h][w]), bias);
      bias = fmaf(-(bf_hi(hi) + bf_hi(lo)), bf_hi(kzr[h][w]), bias);
    }
  bias += dpp<XOR1>(bias); bias += dpp<XOR2>(bias); bias = xrow_sum(bias);

  float m = -INFINITY, lsum = 0.f, zsum = 0.f;
  f32x4 acc[8];
#pragma unroll
  for (int i = 0; i < 8; ++i) acc[i] = f32x4{0.f, 0.f, 0.f, 0.f};

  for (int it = 0; it < NTILE; ++it) {
    const bool more = it + 1 < NTILE;
    const int nxt = more ? (it + 1) * TILE : it * TILE;
    const u32x4 vsc = vsn, vzc = vzn;
    float s[8];
#pragma unroll
    for (int j = 0; j < 8; ++j) {
      u32x4 a;
      a[0] = pk_bf16((float)K[2 * j][0], (float)K[2 * j][1]);         a[1] = pk_bf16((float)K[2 * j][2], (float)K[2 * j][3]);
      a[2] = pk_bf16((float)K[2 * j + 1][0], (float)K[2 * j + 1][1]); a[3] = pk_bf16((float)K[2 * j + 1][2], (float)K[2 * j + 1][3]);
      if (more) { K[2 * j] = ldkv(kp + (size_t)nxt * D + j * D); K[2 * j + 1] = ldkv(kp + (size_t)nxt * D + j * D + 64); }
      f32x4 d = mfma16(a, Qh, f32x4{0.f, 0.f, 0.f, 0.f});
      d = mfma16(a, Ql, d);
      float x = cq == 0 ? d[0] : cq == 1 ? d[1] : cq == 2 ? d[2] : d[3];
      x += dpp<XOR1>(x); x += dpp<XOR2>(x);
      s[j] = x;
    }
    if (more) { vsn = *reinterpret_cast<const u32x4*>(vsp + nxt); vzn = *reinterpret_cast<const u32x4*>(vzp + nxt); }
    float tm = fmaxf(fmaxf(fmaxf(s[0], s[1]), fmaxf(s[2], s[3])), fmaxf(fmaxf(s[4], s[5]), fmaxf(s[6], s[7])));
    tm = xrow_max(tm);
    const float mn = fmaxf(m, tm);
    const float alpha = __builtin_amdgcn_exp2f(m - mn);
    m = mn;
    float w[8];
    float ls = 0.f, zs = 0.f;
#pragma unroll
    for (int j = 0; j < 8; ++j) {
      const float p = __builtin_amdgcn_exp2f(s[j] - mn);
      const float sc = (j & 1) ? bf_hi(vsc[j >> 1]) : bf_lo(vsc[j >> 1]);
      const float zz = (j & 1) ? bf_hi(vzc[j >> 1]) : bf_lo(vzc[j >> 1]);
      w[j] = p * sc; ls += p; zs = fmaf(w[j], zz, zs);
    }
    lsum = fmaf(lsum, alpha, ls); zsum = fmaf(zsum, alpha, zs);
    u32x4 P;
#pragma unroll
    for (int j = 0; j < 4; ++j) {
      const unsigned hi = pk_bf16(w[2 * j], w[2 * j + 1]);
      const unsigned lo = pk_bf16(w[2 * j] - bf_lo(hi), w[2 * j + 1] - bf_hi(hi));
      P[j] = (cq & 1) ? lo : hi;
    }
    {
      int ai = __builtin_bit_cast(int, alpha), ag = ai;
      ag = __builtin_amdgcn_update_dpp(ag, ai, 0x150, 0x1, 0xf, false);
      ag = __builtin_amdgcn_update_dpp(ag, ai, 0x154, 0x2, 0xf, false);
      ag = __builtin_amdgcn_update_dpp(ag, ai, 0x158, 0x4, 0xf, false);
      ag = __builtin_amdgcn_update_dpp(ag, ai, 0x15C, 0x8, 0xf, false);
      const float af = __builtin_bit_cast(float, ag);
#pragma unroll
      for (int i = 0; i < 8; ++i) { acc[i][0] *= af; acc[i][1] *= af; }
    }
    u32x4 B[8];
#pragma unroll
    for (int jp = 0; jp < 4; ++jp) {
#pragma unroll
      for (int h = 0; h < 2; ++h)
#pragma unroll
        for (int e = 0; e < 4; ++e) B[4 * h + e][jp] = pk_bf16((float)V[4 * jp + h][e], (float)V[4 * jp + 2 + h][e]);
      if (more) {
#pragma unroll
        for (int u = 0; u < 2; ++u) { V[4 * jp + 2 * u] = ldkv(vp + (size_t)nxt * D + (2 * jp + u) * D); V[4 * jp + 2 * u + 1] = ldkv(vp + (size_t)nxt * D + (2 * jp + u) * D + 64); }
      }
    }
#pragma unroll
    for (int i = 0; i < 8; ++i) acc[i] = mfma16(P, B[i], acc[i]);
  }

  lsum = xrow_sum(lsum); zsum = xrow_sum(zsum);
  float* rec = lds + wave * REC;
  if (lane < 16 && cq == 0) { rec[hq] = m + bias; rec[4 + hq] = lsum; rec[8 + hq] = zsum; }
#pragma unroll
  for (int h = 0; h < 2; ++h) {
    f32x4 o;
#pragma unroll
    for (int e = 0; e < 4; ++e) o[e] = acc[4 * h + e][0] + acc[4 * h + e][1];
    *reinterpret_cast<f32x4*>(rec + 16 + g * D + 64 * h + 4 * (lane & 15)) = o;
  }
  __syncthreads();
  for (int o = tid; o < GQ * D; o += NTHR) {
    const int h = o >> 7, d = o & 127;
    float M = -INFINITY;
#pragma unroll
    for (int wv = 0; wv < WAVES; ++wv) M = fmaxf(M, lds[wv * REC + h]);
    float L = 0.f, O = 0.f;
#pragma unroll
    for (int wv = 0; wv < WAVES; ++wv) {
      const float f = __builtin_amdgcn_exp2f(lds[wv * REC + h] - M);
      L = fmaf(f, lds[wv * REC + 4 + h], L);
      O = fmaf(f, lds[wv * REC + 16 + o] - lds[wv * REC + 8 + h], O);
    }
    const int row = (hk * GQ + h) * NSPLIT + sp;
    po[(size_t)row * D + d] = O;
    if (d == 0) { pml[2 * row] = M; pml[2 * row + 1] = L; }
  }
}

__global__ void __launch_bounds__(128) attn_combine(const float* __restrict__ po, const float* __restrict__ pml,
                                                    __hip_bfloat16* __restrict__ out) {
  const int h = blockIdx.x, d = threadIdx.x, lane = threadIdx.x & 63;
  float o[NSPLIT];
  f32x2 ml[(NSPLIT + 63) / 64];
#pragma unroll
  for (int i = 0; i < (NSPLIT + 63) / 64; ++i) {
    const int s = lane + 64 * i;
    ml[i] = s < NSPLIT ? *reinterpret_cast<const f32x2*>(pml + 2 * (h * NSPLIT + s)) : f32x2{-INFINITY, 0.f};
  }
#pragma unroll
  for (int s = 0; s < NSPLIT; ++s) o[s] = po[(size_t)(h * NSPLIT + s) * D + d];
  float M = -INFINITY;
#pragma unroll
  for (int i = 0; i < (NSPLIT + 63) / 64; ++i) M = fmaxf(M, ml[i][0]);
  M = fmaxf(M, dpp<XOR1>(M)); M = fmaxf(M, dpp<XOR2>(M)); M = fmaxf(M, dpp<ROR4>(M)); M = fmaxf(M, dpp<ROR8>(M)); M = xrow_max(M);
  float f[(NSPLIT + 63) / 64], L = 0.f;
#pragma unroll
  for (int i = 0; i < (NSPLIT + 63) / 64; ++i) { f[i] = __builtin_amdgcn_exp2f(ml[i][0] - M); L = fmaf(f[i], ml[i][1], L); }
  L += dpp<XOR1>(L); L += dpp<XOR2>(L); L += dpp<0x141>(L); L += dpp<0x140>(L); L = xrow_sum(L);
  float O = 0.f;
#pragma unroll
  for (int s = 0; s < NSPLIT; ++s) O = fmaf(__builtin_bit_cast(float, __builtin_amdgcn_readlane(__builtin_bit_cast(int, f[s / 64]), s & 63)), o[s], O);
  out[h * D + d] = __float2bfloat16(O / L);
}

}

extern "C" void kernel_launch(void* const* d_in, const int* in_sizes, int n_in, void* d_out, int out_size, void* d_ws,
                              size_t ws_size, hipStream_t stream) {
  const unsigned short* q  = (const unsigned short*)d_in[0];
  const int* qk            = (const int*)d_in[1];
  const int* qv            = (const int*)d_in[2];
  const unsigned short* ks = (const unsigned short*)d_in[3];
  const unsigned short* kz = (const unsigned short*)d_in[4];
  const unsigned short* vs = (const unsigned short*)d_in[5];
  const unsigned short* vz = (const unsigned short*)d_in[6];
  float* po  = (float*)d_ws;
  float* pml = po + (size_t)HQ * NSPLIT * D;
  attn_split<<<HKV * NSPLIT, NTHR, 0, stream>>>(q, qk, qv, ks, kz, vs, vz, po, pml);
  attn_combine<<<HQ, 128, 0, stream>>>(po, pml, (__hip_bfloat16*)d_out);
}
```

```cpp
#include <hip/hip_runtime.h>
#include <hip/hip_bf16.h>
#include <stdint.h>

#define K_NT 1
#define V_NT 1
#define WAVES 8
#define NSPLIT 32

namespace {

constexpr int HQ = 32, HKV = 8, GQ = HQ / HKV, SKV = 32768, D = 128, NGK = 2, GTOK = SKV / NGK;
constexpr int NTHR = WAVES * 64;
constexpr int TOK_WG = SKV / NSPLIT;
constexpr int TOK_WAVE = TOK_WG / WAVES;
constexpr int TILE = 32;
constexpr int NTILE = TOK_WAVE / TILE;
static_assert(GQ == 4 && TOK_WAVE % TILE == 0 && GTOK % TOK_WAVE == 0, "shape");

typedef int    i32x4 __attribute__((ext_vector_type(4)));
typedef unsigned u32x4 __attribute__((ext_vector_type(4)));
typedef unsigned u32x2 __attribute__((ext_vector_type(2)));
typedef float  f32x4 __attribute__((ext_vector_type(4)));
typedef float  f32x2 __attribute__((ext_vector_type(2)));
typedef __bf16 bf16x8 __attribute__((ext_vector_type(8)));
typedef __bf16 bf16x2 __attribute__((ext_vector_type(2)));

template <int NT> __device__ __forceinline__ i32x4 ldp(const int* p) {
  if constexpr (NT) return __builtin_nontemporal_load(reinterpret_cast<const i32x4*>(p));
  else return *reinterpret_cast<const i32x4*>(p);
}
#define ldk(p) ldp<K_NT>(p)
#define ldv(p) ldp<V_NT>(p)
__device__ __forceinline__ unsigned pk_bf16(float lo, float hi) {
  f32x2 f = {lo, hi};
  bf16x2 b = __builtin_convertvector(f, bf16x2);
  return __builtin_bit_cast(unsigned, b);
}
__device__ __forceinline__ float bf_lo(unsigned u) { return __builtin_bit_cast(float, u << 16); }
__device__ __forceinline__ float bf_hi(unsigned u) { return __builtin_bit_cast(float, u & 0xffff0000u); }
template <int CTRL> __device__ __forceinline__ float dpp(float x) {
  return __builtin_bit_cast(float, __builtin_amdgcn_mov_dpp(__builtin_bit_cast(int, x), CTRL, 0xf, 0xf, true));
}
constexpr int XOR1 = 0xB1, XOR2 = 0x4E, ROR4 = 0x124, ROR8 = 0x128;
__device__ __forceinline__ float xrow_max(float x) {
  auto s = __builtin_amdgcn_permlane16_swap(__float_as_uint(x), __float_as_uint(x), false, false);
  x = fmaxf(__uint_as_float(s[0]), __uint_as_float(s[1]));
  auto t = __builtin_amdgcn_permlane32_swap(__float_as_uint(x), __float_as_uint(x), false, false);
  return fmaxf(__uint_as_float(t[0]), __uint_as_float(t[1]));
}
__device__ __forceinline__ float xrow_sum(float x) {
  auto s = __builtin_amdgcn_permlane16_swap(__float_as_uint(x), __float_as_uint(x), false, false);
  x = __uint_as_float(s[0]) + __uint_as_float(s[1]);
  auto t = __builtin_amdgcn_permlane32_swap(__float_as_uint(x), __float_as_uint(x), false, false);
  return __uint_as_float(t[0]) + __uint_as_float(t[1]);
}
__device__ __forceinline__ f32x4 mfma16(u32x4 a, u32x4 b, f32x4 c) {
  return __builtin_amdgcn_mfma_f32_16x16x32_bf16(__builtin_bit_cast(bf16x8, a), __builtin_bit_cast(bf16x8, b), c, 0, 0, 0);
}
#define SB() __builtin_amdgcn_sched_barrier(0)

constexpr int REC = 16 + GQ * D;

__global__ void __launch_bounds__(NTHR, 2) attn_split(const unsigned short* __restrict__ q, const int* __restrict__ qk,
                                                      const int* __restrict__ qv, const unsigned short* __restrict__ ks,
                                                      const unsigned short* __restrict__ kz, const unsigned short* __restrict__ vs,
                                                      const unsigned short* __restrict__ vz, float* __restrict__ po,
                                                      float* __restrict__ pml) {
  __shared__ __attribute__((aligned(16))) float lds[WAVES * REC];
  const int tid = threadIdx.x, lane = tid & 63;
  const int wave = __builtin_amdgcn_readfirstlane(tid >> 6);
  const int bid = blockIdx.x;
  const int hk = bid / NSPLIT, sp = bid % NSPLIT;
  const int tok0 = sp * TOK_WG + wave * TOK_WAVE;
  const int grp = tok0 / GTOK;
  const int g = lane >> 4;

  const int hq = (lane >> 2) & 3, cq = lane & 3;
  const int dq = 16 * g + 4 * cq;
  const bool c1 = (lane & 1) != 0, c2 = (lane & 2) != 0;
  u32x2 qr[2], ksr[2], kzr[2];
#pragma unroll
  for (int h = 0; h < 2; ++h) {
    qr[h]  = *reinterpret_cast<const u32x2*>(q + (hk * GQ + hq) * D + 64 * h + dq);
    ksr[h] = *reinterpret_cast<const u32x2*>(ks + (hk * NGK + grp) * D + 64 * h + dq);
    kzr[h] = *reinterpret_cast<const u32x2*>(kz + (hk * NGK + grp) * D + 64 * h + dq);
  }
  const unsigned short* vsp = vs + (size_t)hk * SKV + tok0 + 8 * g;
  const unsigned short* vzp = vz + (size_t)hk * SKV + tok0 + 8 * g;
  u32x4 vsn = *reinterpret_cast<const u32x4*>(vsp);
  u32x4 vzn = *reinterpret_cast<const u32x4*>(vzp);
  SB();
  const int* kp = qk + ((size_t)hk * SKV + tok0 + 8 * hq) * D + dq;
  const int* vp = qv + ((size_t)hk * SKV + tok0 + 8 * g) * D + 4 * (lane & 15);
  i32x4 K[16], V[16];
#pragma unroll
  for (int j = 0; j < 8; ++j) { K[2 * j] = ldk(kp + j * D); K[2 * j + 1] = ldk(kp + j * D + 64); }
  SB();
#pragma unroll
  for (int j = 0; j < 8; ++j) { V[2 * j] = ldv(vp + j * D); V[2 * j + 1] = ldv(vp + j * D + 64); }
  SB();

  constexpr float C2 = 0.088388347648318440f * 1.4426950408889634f;
  u32x4 Qh, Ql; float bias = 0.f;
#pragma unroll
  for (int h = 0; h < 2; ++h)
#pragma unroll
    for (int w = 0; w < 2; ++w) {
      const float a0 = bf_lo(qr[h][w]) * bf_lo(ksr[h][w]) * C2, a1 = bf_hi(qr[h][w]) * bf_hi(ksr[h][w]) * C2;
      const unsigned hi = pk_bf16(a0, a1);
      const unsigned lo = pk_bf16(a0 - bf_lo(hi), a1 - bf_hi(hi));
      Qh[2 * h + w] = hi; Ql[2 * h + w] = lo;
      bias = fmaf(-(bf_lo(hi) + bf_lo(lo)), bf_lo(kzr[h][w]), bias);
      bias = fmaf(-(bf_hi(hi) + bf_hi(lo)), bf_hi(kzr[h][w]), bias);
    }
  bias += dpp<XOR1>(bias); bias += dpp<XOR2>(bias); bias = xrow_sum(bias);

  float m = -INFINITY, lsum = 0.f, zsum = 0.f;
  f32x4 acc[8];
#pragma unroll
  for (int i = 0; i < 8; ++i) acc[i] = f32x4{0.f, 0.f, 0.f, 0.f};

  for (int it = 0; it < NTILE; ++it) {
    const bool more = it + 1 < NTILE;
    const int nxt = more ? (it + 1) * TILE : it * TILE;
    const u32x4 vsc = vsn, vzc = vzn;
    float s[8];
#pragma unroll
    for (int j = 0; j < 8; ++j) {
      u32x4 a;
      a[0] = pk_bf16((float)K[2 * j][0], (float)K[2 * j][1]);         a[1] = pk_bf16((float)K[2 * j][2], (float)K[2 * j][3]);
      a[2] = pk_bf16((float)K[2 * j + 1][0], (float)K[2 * j + 1][1]); a[3] = pk_bf16((float)K[2 * j + 1][2], (float)K[2 * j + 1][3]);
      if (more) { K[2 * j] = ldk(kp + (size_t)nxt * D + j * D); K[2 * j + 1] = ldk(kp + (size_t)nxt * D + j * D + 64); }
      f32x4 d = mfma16(a, Qh, f32x4{0.f, 0.f, 0.f, 0.f});
      d = mfma16(a, Ql, d);
      const float x0 = c1 ? d[1] : d[0], x1 = c1 ? d[3] : d[2];
      float x = c2 ? x1 : x0;
      x += dpp<XOR1>(x); x += dpp<XOR2>(x);
      s[j] = x;
    }
    if (more) { vsn = *reinterpret_cast<const u32x4*>(vsp + nxt); vzn = *reinterpret_cast<const u32x4*>(vzp + nxt); }
    float tm = fmaxf(fmaxf(fmaxf(s[0], s[1]), fmaxf(s[2], s[3])), fmaxf(fmaxf(s[4], s[5]), fmaxf(s[6], s[7])));
    tm = xrow_max(tm);
    const float mn = fmaxf(m, tm);
    const float alpha = __builtin_amdgcn_exp2f(m - mn);
    m = mn;
    float w[8];
    float ls = 0.f, zs = 0.f;
#pragma unroll
    for (int j = 0; j < 8; ++j) {
      const float p = __builtin_amdgcn_exp2f(s[j] - mn);
      const float sc = (j & 1) ? bf_hi(vsc[j >> 1]) : bf_lo(vsc[j >> 1]);
      const float zz = (j & 1) ? bf_hi(vzc[j >> 1]) : bf_lo(vzc[j >> 1]);
      w[j] = p * sc; ls += p; zs = fmaf(w[j], zz, zs);
    }
    lsum = fmaf(lsum, alpha, ls); zsum = fmaf(zsum, alpha, zs);
    u32x4 P;
#pragma unroll
    for (int j = 0; j < 4; ++j) {
      const unsigned hi = pk_bf16(w[2 * j], w[2 * j + 1]);
      const unsigned lo = pk_bf16(w[2 * j] - bf_lo(hi), w[2 * j + 1] - bf_hi(hi));
      P[j] = c1 ? lo : hi;
    }
    {
      int ai = __builtin_bit_cast(int, alpha), ag = ai;
      ag = __builtin_amdgcn_update_dpp(ag, ai, 0x150, 0x1, 0xf, false);
      ag = __builtin_amdgcn_update_dpp(ag, ai, 0x154, 0x2, 0xf, false);
      ag = __builtin_amdgcn_update_dpp(ag, ai, 0x158, 0x4, 0xf, false);
      ag = __builtin_amdgcn_update_dpp(ag, ai, 0x15C, 0x8, 0xf, false);
      const float af = __builtin_bit_cast(float, ag);
#pragma unroll
      for (int i = 0; i < 8; ++i) { acc[i][0] *= af; acc[i][1] *= af; }
    }
    u32x4 B[8];
#pragma unroll
    for (int jp = 0; jp < 4; ++jp) {
#pragma unroll
      for (int h = 0; h < 2; ++h)
#pragma unroll
        for (int e = 0; e < 4; ++e) B[4 * h + e][jp] = pk_bf16((float)V[4 * jp + h][e], (float)V[4 * jp + 2 + h][e]);
      if (more) {
#pragma unroll
        for (int u = 0; u < 2; ++u) { V[4 * jp + 2 * u] = ldv(vp + (size_t)nxt * D + (2 * jp + u) * D); V[4 * jp + 2 * u + 1] = ldv(vp + (size_t)nxt * D + (2 * jp + u) * D + 64); }
      }
    }
#pragma unroll
    for (int i = 0; i < 8; ++i) acc[i] = mfma16(P, B[i], acc[i]);
  }

  lsum = xrow_sum(lsum); zsum = xrow_sum(zsum);
  float* rec = lds + wave * REC;
  if (lane < 16 && cq == 0) { rec[hq] = m + bias; rec[4 + hq] = lsum; rec[8 + hq] = zsum; }
#pragma unroll
  for (int h = 0; h < 2; ++h) {
    f32x4 o;
#pragma unroll
    for (int e = 0; e < 4; ++e) o[e] = acc[4 * h + e][0] + acc[4 * h + e][1];
    *reinterpret_cast<f32x4*>(rec + 16 + g * D + 64 * h + 4 * (lane & 15)) = o;
  }
  __syncthreads();
  for (int o = tid; o < GQ * D; o += NTHR) {
    const int h = o >> 7, d = o & 127;
    float M = -INFINITY;
#pragma unroll
    for (int wv = 0; wv < WAVES; ++wv) M = fmaxf(M, lds[wv * REC + h]);
    float L = 0.f, O = 0.f;
#pragma unroll
    for (int wv = 0; wv < WAVES; ++wv) {
      const float f = __builtin_amdgcn_exp2f(lds[wv * REC + h] - M);
      L = fmaf(f, lds[wv * REC + 4 + h], L);
      O = fmaf(f, lds[wv * REC + 16 + o] - lds[wv * REC + 8 + h], O);
    }
    const int row = (hk * GQ + h) * NSPLIT + sp;
    po[(size_t)row * D + d] = O;
    if (d == 0) { pml[2 * row] = M; pml[2 * row + 1] = L; }
  }
}

__global__ void __launch_bounds__(128) attn_combine(const float* __restrict__ po, const float* __restrict__ pml,
                                                    __hip_bfloat16* __restrict__ out) {
  const int h = blockIdx.x, d = threadIdx.x, lane = threadIdx.x & 63;
  float o[NSPLIT];
  f32x2 ml[(NSPLIT + 63) / 64];
#pragma unroll
  for (int i = 0; i < (NSPLIT + 63) / 64; ++i) {
    const int s = lane + 64 * i;
    ml[i] = s < NSPLIT ? *reinterpret_cast<const f32x2*>(pml + 2 * (h * NSPLIT + s)) : f32x2{-INFINITY, 0.f};
  }
#pragma unroll
  for (int s = 0; s < NSPLIT; ++s) o[s] = po[(size_t)(h * NSPLIT + s) * D + d];
  float M = -INFINITY;
#pragma unroll
  for (int i = 0; i < (NSPLIT + 63) / 64; ++i) M = fmaxf(M, ml[i][0]);
  M = fmaxf(M, dpp<XOR1>(M)); M = fmaxf(M, dpp<XOR2>(M)); M = fmaxf(M, dpp<ROR4>(M)); M = fmaxf(M, dpp<ROR8>(M)); M = xrow_max(M);
  float f[(NSPLIT + 63) / 64], L = 0.f;
#pragma unroll
  for (int i = 0; i < (NSPLIT + 63) / 64; ++i) { f[i] = __builtin_amdgcn_exp2f(ml[i][0] - M); L = fmaf(f[i], ml[i][1], L); }
  L += dpp<XOR1>(L); L += dpp<XOR2>(L); L += dpp<0x141>(L); L += dpp<0x140>(L); L = xrow_sum(L);
  float O = 0.f;
#pragma unroll
  for (int s = 0; s < NSPLIT; ++s) O = fmaf(__builtin_bit_cast(float, __builtin_amdgcn_readlane(__builtin_bit_cast(int, f[s / 64]), s & 63)), o[s], O);
  out[h * D + d] = __float2bfloat16(O / L);
}

}

extern "C" void kernel_launch(void* const* d_in, const int* in_sizes, int n_in, void* d_out, int out_size, void* d_ws,
                              size_t ws_size, hipStream_t stream) {
  const unsigned short* q  = (const unsigned short*)d_in[0];
  const int* qk            = (const int*)d_in[1];
  const int* qv            = (const int*)d_in[2];
  const unsigned short* ks = (const unsigned short*)d_in[3];
  const unsigned short* kz = (const unsigned short*)d_in[4];
  const unsigned short* vs = (const unsigned short*)d_in[5];
  const unsigned short* vz = (const unsigned short*)d_in[6];
  float* po  = (float*)d_ws;
  float* pml = po + (size_t)HQ * NSPLIT * D;
  attn_split<<<HKV * NSPLIT, NTHR, 0, stream>>>(q, qk, qv, ks, kz, vs, vz, po, pml);
  attn_combine<<<HQ, 128, 0, stream>>>(po, pml, (__hip_bfloat16*)d_out);
}
```

```cpp
#include <hip/hip_runtime.h>
#include <hip/hip_bf16.h>
#include <stdint.h>

#define WAVES 8
#define STAMP_OFF (64u << 20)
#define STAMP_N (HKV * NSPLIT * WAVES)

namespace {

constexpr int HQ = 32, HKV = 8, GQ = HQ / HKV, SKV = 32768, D = 128, NGK = 2, GTOK = SKV / NGK;
constexpr int NSPLIT = 32;
constexpr int NTHR = WAVES * 64;
constexpr int TOK_WG = SKV / NSPLIT;
constexpr int TILE = 32;
constexpr int NTILE = TOK_WG / (WAVES * TILE);
static_assert(GQ == 4 && TOK_WG % (WAVES * TILE) == 0 && GTOK % TOK_WG == 0 && WAVES >= 4, "shape");

constexpr int CTL_BYTES = 4096;
constexpr int PREC = GQ * D + 8;
constexpr unsigned long long MASK48 = 0xFFFFFFFFFFFFull;

typedef unsigned u32x4 __attribute__((ext_vector_type(4)));
typedef unsigned u32x2 __attribute__((ext_vector_type(2)));
typedef float  f32x4 __attribute__((ext_vector_type(4)));
typedef float  f32x2 __attribute__((ext_vector_type(2)));
typedef __bf16 bf16x8 __attribute__((ext_vector_type(8)));
typedef __bf16 bf16x2 __attribute__((ext_vector_type(2)));
typedef __attribute__((address_space(1))) unsigned long long gu64;

__device__ __forceinline__ unsigned pk_bf16(float lo, float hi) {
  f32x2 f = {lo, hi};
  bf16x2 b = __builtin_convertvector(f, bf16x2);
  return __builtin_bit_cast(unsigned, b);
}
__device__ __forceinline__ unsigned pk_int(unsigned lo, unsigned hi) { return pk_bf16((float)(int)lo, (float)(int)hi); }
__device__ __forceinline__ float bf_lo(unsigned u) { return __builtin_bit_cast(float, u << 16); }
__device__ __forceinline__ float bf_hi(unsigned u) { return __builtin_bit_cast(float, u & 0xffff0000u); }
template <int CTRL> __device__ __forceinline__ float dpp(float x) {
  return __builtin_bit_cast(float, __builtin_amdgcn_mov_dpp(__builtin_bit_cast(int, x), CTRL, 0xf, 0xf, true));
}
constexpr int XOR1 = 0xB1, XOR2 = 0x4E, HMIRROR = 0x141, MIRROR = 0x140;
__device__ __forceinline__ float x16_max(float x) {
  auto s = __builtin_amdgcn_permlane16_swap(__float_as_uint(x), __float_as_uint(x), false, false);
  return fmaxf(__uint_as_float(s[0]), __uint_as_float(s[1]));
}
__device__ __forceinline__ float x32_max(float x) {
  auto t = __builtin_amdgcn_permlane32_swap(__float_as_uint(x), __float_as_uint(x), false, false);
  return fmaxf(__uint_as_float(t[0]), __uint_as_float(t[1]));
}
__device__ __forceinline__ float x16_sum(float x) {
  auto s = __builtin_amdgcn_permlane16_swap(__float_as_uint(x), __float_as_uint(x), false, false);
  return __uint_as_float(s[0]) + __uint_as_float(s[1]);
}
__device__ __forceinline__ float x32_sum(float x) {
  auto t = __builtin_amdgcn_permlane32_swap(__float_as_uint(x), __float_as_uint(x), false, false);
  return __uint_as_float(t[0]) + __uint_as_float(t[1]);
}
__device__ __forceinline__ unsigned rfl(unsigned x) { return (unsigned)__builtin_amdgcn_readfirstlane((int)x); }
__device__ __forceinline__ f32x4 mfma16(u32x4 a, u32x4 b, f32x4 c) {
  return __builtin_amdgcn_mfma_f32_16x16x32_bf16(__builtin_bit_cast(bf16x8, a), __builtin_bit_cast(bf16x8, b), c, 0, 0, 0);
}
__device__ __forceinline__ u32x4 make_srd(const void* p, unsigned bytes) {
  return u32x4{(unsigned)(size_t)p, (unsigned)((size_t)p >> 32) & 0xffffu, bytes, 0x00020000u};
}
template <int OFF> __device__ __forceinline__ void ld_nt(u32x4& dst, unsigned voff, u32x4 srd, unsigned soff) {
  asm volatile("buffer_load_dwordx4 %0, %1, %2, %3 offen offset:%4 nt" : "=v"(dst) : "v"(voff), "s"(srd), "s"(soff), "i"(OFF));
}
__device__ __forceinline__ void ld_x4(u32x4& dst, unsigned voff, u32x4 srd, unsigned soff) {
  asm volatile("buffer_load_dwordx4 %0, %1, %2, %3 offen" : "=v"(dst) : "v"(voff), "s"(srd), "s"(soff));
}
template <int OFF> __device__ __forceinline__ void ld_x2(u32x2& dst, unsigned voff, u32x4 srd, unsigned soff) {
  asm volatile("buffer_load_dwordx2 %0, %1, %2, %3 offen offset:%4" : "=v"(dst) : "v"(voff), "s"(srd), "s"(soff), "i"(OFF));
}
#define WAIT2(N, a, b) asm volatile("s_waitcnt vmcnt(" #N ")" : "+v"(a), "+v"(b))
#define WAIT4(N, a, b, c, d) asm volatile("s_waitcnt vmcnt(" #N ")" : "+v"(a), "+v"(b), "+v"(c), "+v"(d))
#define PIN(x) asm volatile("" : "+v"(x))

constexpr int REC = 16 + GQ * D;

__global__ void __launch_bounds__(NTHR, WAVES / 4) attn_decode(const unsigned short* __restrict__ q, const int* __restrict__ qk,
                                                               const int* __restrict__ qv, const unsigned short* __restrict__ ks,
                                                               const unsigned short* __restrict__ kz, const unsigned short* __restrict__ vs,
                                                               const unsigned short* __restrict__ vz, unsigned char* __restrict__ ws,
                                                               unsigned short* __restrict__ out) {
  __shared__ __attribute__((aligned(16))) float lds[WAVES * REC + 4];
  const int tid = threadIdx.x, lane = tid & 63;
  const int wave = __builtin_amdgcn_readfirstlane(tid >> 6);
  const int bid = blockIdx.x;
  const int hk = bid / NSPLIT, sp = bid % NSPLIT;
  const int grp = sp * TOK_WG / GTOK;
  const int g = lane >> 4, hq = (lane >> 2) & 3, cq = lane & 3;
  const bool c1 = (lane & 1) != 0, c2 = (lane & 2) != 0;

  u32x2 qr0, qr1, ks0, ks1, kz0, kz1;
  u32x4 ctl;
  {
    const unsigned dq2 = (16 * g + 4 * cq) * 2;
    const u32x4 sq = make_srd(q, HQ * D * 2), sks = make_srd(ks, HKV * NGK * D * 2), skz = make_srd(kz, HKV * NGK * D * 2);
    const unsigned zero = 0;
    asm volatile("buffer_load_dwordx4 %0, %1, %2, %3 offen sc1" : "=v"(ctl) : "v"(zero), "s"(make_srd(ws, CTL_BYTES)), "s"(hk * 256));
    ld_x2<0>(qr0, dq2 + hq * (D * 2), sq, hk * (GQ * D * 2));   ld_x2<128>(qr1, dq2 + hq * (D * 2), sq, hk * (GQ * D * 2));
    ld_x2<0>(ks0, dq2, sks, (hk * NGK + grp) * (D * 2));        ld_x2<128>(ks1, dq2, sks, (hk * NGK + grp) * (D * 2));
    ld_x2<0>(kz0, dq2, skz, (hk * NGK + grp) * (D * 2));        ld_x2<128>(kz1, dq2, skz, (hk * NGK + grp) * (D * 2));
  }
  u32x4 sk = make_srd(qk, HKV * SKV * D * 4u), sv = make_srd(qv, HKV * SKV * D * 4u);
  u32x4 svs = make_srd(vs, HKV * SKV * 2u), svz = make_srd(vz, HKV * SKV * 2u);
  const unsigned kofs = hq * (8 * D * 4) + g * 64 + cq * 16, vofs = g * (8 * D * 4) + (lane & 15) * 16, sofs = g * 16;
  unsigned row = hk * SKV + sp * TOK_WG + wave * TILE;
  u32x4 K[16], V[16], S, Z;
#define KLD(j) do { ld_nt<(j) * 512>(K[2 * (j)], kofs, sk, row * (D * 4)); ld_nt<(j) * 512 + 256>(K[2 * (j) + 1], kofs, sk, row * (D * 4)); } while (0)
#define VLD(j) do { ld_nt<(j) * 512>(V[2 * (j)], vofs, sv, row * (D * 4)); ld_nt<(j) * 512 + 256>(V[2 * (j) + 1], vofs, sv, row * (D * 4)); } while (0)
  KLD(0); KLD(1); KLD(2); KLD(3); KLD(4); KLD(5); KLD(6); KLD(7);
  ld_x4(S, sofs, svs, row * 2); ld_x4(Z, sofs, svz, row * 2);
  VLD(0); VLD(1); VLD(2); VLD(3); VLD(4); VLD(5); VLD(6); VLD(7);

  asm volatile("s_waitcnt vmcnt(34)" : "+v"(ctl), "+v"(qr0), "+v"(qr1), "+v"(ks0), "+v"(ks1), "+v"(kz0), "+v"(kz1));
  const unsigned long long epoch = ((unsigned long long)rfl(ctl[1]) << 32) | rfl(ctl[0]);
  const unsigned long long arr0  = ((unsigned long long)rfl(ctl[3]) << 32) | rfl(ctl[2]);
  const unsigned long long tag = (epoch * 0x5DEECE66Dull + 0xBull) & MASK48;
  gu64* const p_epoch = (gu64*)(ws + hk * 256);
  gu64* const p_arr = p_epoch + 1;
  if (tid == 0 && (arr0 >> 16) != tag) {
    unsigned long long expect = arr0;
    __hip_atomic_compare_exchange_strong(p_arr, &expect, tag << 16, __ATOMIC_RELAXED, __ATOMIC_RELAXED, __HIP_MEMORY_SCOPE_AGENT);
  }
  constexpr float C2 = 0.088388347648318440f * 1.4426950408889634f;
  u32x4 Qh, Ql; float bias = 0.f;
  {
    const unsigned qq[4] = {qr0[0], qr0[1], qr1[0], qr1[1]}, kk[4] = {ks0[0], ks0[1], ks1[0], ks1[1]}, zz[4] = {kz0[0], kz0[1], kz1[0], kz1[1]};
#pragma unroll
    for (int w = 0; w < 4; ++w) {
      const float a0 = bf_lo(qq[w]) * bf_lo(kk[w]) * C2, a1 = bf_hi(qq[w]) * bf_hi(kk[w]) * C2;
      const unsigned hi = pk_bf16(a0, a1);
      const unsigned lo = pk_bf16(a0 - bf_lo(hi), a1 - bf_hi(hi));
      Qh[w] = hi; Ql[w] = lo;
      bias = fmaf(-(bf_lo(hi) + bf_lo(lo)), bf_lo(zz[w]), bias);
      bias = fmaf(-(bf_hi(hi) + bf_hi(lo)), bf_hi(zz[w]), bias);
    }
  }
  bias += dpp<XOR1>(bias); bias += dpp<XOR2>(bias); bias = x32_sum(x16_sum(bias));

  float m = -INFINITY, lsum = 0.f, zsum = 0.f;
  f32x4 acc[8];
#pragma unroll
  for (int i = 0; i < 8; ++i) acc[i] = f32x4{0.f, 0.f, 0.f, 0.f};

#pragma unroll 1
  for (int it = 0; it < NTILE; ++it) {
    if (it + 1 < NTILE) row += WAVES * TILE;
    else { sk[2] = 0; sv[2] = 0; svs[2] = 0; svz[2] = 0; }
    float s[8];
#define QK(j) do { \
      WAIT2(32, K[2 * (j)], K[2 * (j) + 1]); \
      u32x4 a = {pk_int(K[2 * (j)][0], K[2 * (j)][1]), pk_int(K[2 * (j)][2], K[2 * (j)][3]), \
                 pk_int(K[2 * (j) + 1][0], K[2 * (j) + 1][1]), pk_int(K[2 * (j) + 1][2], K[2 * (j) + 1][3])}; \
      PIN(a); \
      KLD(j); \
      f32x4 d = mfma16(a, Qh, f32x4{0.f, 0.f, 0.f, 0.f}); \
      d = mfma16(a, Ql, d); \
      const float x0 = c1 ? d[1] : d[0], x1 = c1 ? d[3] : d[2]; \
      float x = c2 ? x1 : x0; \
      x += dpp<XOR1>(x); x += dpp<XOR2>(x); \
      s[j] = x; } while (0)
    QK(0); QK(1); QK(2); QK(3); QK(4); QK(5); QK(6); QK(7);
    float tm = fmaxf(fmaxf(fmaxf(s[0], s[1]), fmaxf(s[2], s[3])), fmaxf(fmaxf(s[4], s[5]), fmaxf(s[6], s[7])));
    tm = x32_max(x16_max(tm));
    const float mn = fmaxf(m, tm);
    const float alpha = __builtin_amdgcn_exp2f(m - mn);
    m = mn;
    WAIT2(32, S, Z);
    float w[8];
    float ls = 0.f, zs = 0.f;
#pragma unroll
    for (int j = 0; j < 8; ++j) {
      const float p = __builtin_amdgcn_exp2f(s[j] - mn);
      const float sc = (j & 1) ? bf_hi(S[j >> 1]) : bf_lo(S[j >> 1]);
      const float zz = (j & 1) ? bf_hi(Z[j >> 1]) : bf_lo(Z[j >> 1]);
      w[j] = p * sc; ls += p; zs = fmaf(w[j], zz, zs);
    }
    lsum = fmaf(lsum, alpha, ls); zsum = fmaf(zsum, alpha, zs);
    u32x4 P;
#pragma unroll
    for (int j = 0; j < 4; ++j) {
      const unsigned hi = pk_bf16(w[2 * j], w[2 * j + 1]);
      const unsigned lo = pk_bf16(w[2 * j] - bf_lo(hi), w[2 * j + 1] - bf_hi(hi));
      P[j] = c1 ? lo : hi;
    }
    asm volatile("" : "+v"(P), "+v"(zsum));
    ld_x4(S, sofs, svs, row * 2); ld_x4(Z, sofs, svz, row * 2);
    {
      int ai = __builtin_bit_cast(int, alpha), ag = ai;
      ag = __builtin_amdgcn_update_dpp(ag, ai, 0x150, 0x1, 0xf, false);
      ag = __builtin_amdgcn_update_dpp(ag, ai, 0x154, 0x2, 0xf, false);
      ag = __builtin_amdgcn_update_dpp(ag, ai, 0x158, 0x4, 0xf, false);
      ag = __builtin_amdgcn_update_dpp(ag, ai, 0x15C, 0x8, 0xf, false);
      const float af = __builtin_bit_cast(float, ag);
#pragma unroll
      for (int i = 0; i < 8; ++i) { acc[i][0] *= af; acc[i][1] *= af; }
    }
    u32x4 B[8];
#define PVC(jp) do { \
      WAIT4(30, V[4 * (jp)], V[4 * (jp) + 1], V[4 * (jp) + 2], V[4 * (jp) + 3]); \
      unsigned b0 = pk_int(V[4 * (jp)][0], V[4 * (jp) + 2][0]), b1 = pk_int(V[4 * (jp)][1], V[4 * (jp) + 2][1]), \
               b2 = pk_int(V[4 * (jp)][2], V[4 * (jp) + 2][2]), b3 = pk_int(V[4 * (jp)][3], V[4 * (jp) + 2][3]), \
               b4 = pk_int(V[4 * (jp) + 1][0], V[4 * (jp) + 3][0]), b5 = pk_int(V[4 * (jp) + 1][1], V[4 * (jp) + 3][1]), \
               b6 = pk_int(V[4 * (jp) + 1][2], V[4 * (jp) + 3][2]), b7 = pk_int(V[4 * (jp) + 1][3], V[4 * (jp) + 3][3]); \
      asm volatile("" : "+v"(b0), "+v"(b1), "+v"(b2), "+v"(b3), "+v"(b4), "+v"(b5), "+v"(b6), "+v"(b7)); \
      VLD(2 * (jp)); VLD(2 * (jp) + 1); \
      B[0][jp] = b0; B[1][jp] = b1; B[2][jp] = b2; B[3][jp] = b3; B[4][jp] = b4; B[5][jp] = b5; B[6][jp] = b6; B[7][jp] = b7; } while (0)
    PVC(0); PVC(1); PVC(2); PVC(3);
#pragma unroll
    for (int i = 0; i < 8; ++i) acc[i] = mfma16(P, B[i], acc[i]);
  }
  asm volatile("s_waitcnt vmcnt(0)" : "+v"(K[0]), "+v"(K[1]), "+v"(K[2]), "+v"(K[3]), "+v"(K[4]), "+v"(K[5]), "+v"(K[6]), "+v"(K[7]),
               "+v"(K[8]), "+v"(K[9]), "+v"(K[10]), "+v"(K[11]), "+v"(K[12]), "+v"(K[13]), "+v"(K[14]), "+v"(K[15]), "+v"(S), "+v"(Z));
  asm volatile("" : "+v"(V[0]), "+v"(V[1]), "+v"(V[2]), "+v"(V[3]), "+v"(V[4]), "+v"(V[5]), "+v"(V[6]), "+v"(V[7]),
               "+v"(V[8]), "+v"(V[9]), "+v"(V[10]), "+v"(V[11]), "+v"(V[12]), "+v"(V[13]), "+v"(V[14]), "+v"(V[15]));

  lsum = x32_sum(x16_sum(lsum)); zsum = x32_sum(x16_sum(zsum));
  float* rec = lds + wave * REC;
  if (lane < 16 && cq == 0) { rec[hq] = m + bias; rec[4 + hq] = lsum; rec[8 + hq] = zsum; }
#pragma unroll
  for (int h = 0; h < 2; ++h) {
    f32x4 o;
#pragma unroll
    for (int e = 0; e < 4; ++e) o[e] = acc[4 * h + e][0] + acc[4 * h + e][1];
    *reinterpret_cast<f32x4*>(rec + 16 + g * D + 64 * h + 4 * (lane & 15)) = o;
  }
  __syncthreads();
  const __amdgpu_buffer_rsrc_t rp = __builtin_amdgcn_make_buffer_rsrc(ws + CTL_BYTES, 0, HKV * NSPLIT * PREC * 4, 0x00020000);
  if (wave < 2) {
    const int h = tid >> 5;
    float M = -INFINITY;
#pragma unroll
    for (int wv = 0; wv < WAVES; ++wv) M = fmaxf(M, lds[wv * REC + h]);
    f32x4 O = {0.f, 0.f, 0.f, 0.f};
#pragma unroll
    for (int wv = 0; wv < WAVES; ++wv) {
      const float f = __builtin_amdgcn_exp2f(lds[wv * REC + h] - M), fz = -f * lds[wv * REC + 8 + h];
      const f32x4 o = *reinterpret_cast<const f32x4*>(lds + wv * REC + 16 + 4 * tid);
#pragma unroll
      for (int e = 0; e < 4; ++e) O[e] += fmaf(f, o[e], fz);
    }
    __builtin_amdgcn_raw_buffer_store_b128(__builtin_bit_cast(u32x4, O), rp, tid * 16, bid * (PREC * 4), 16);
  } else if (wave == 2) {
    const int h = lane & 3;
    float M = -INFINITY;
#pragma unroll
    for (int wv = 0; wv < WAVES; ++wv) M = fmaxf(M, lds[wv * REC + h]);
    float L = 0.f;
#pragma unroll
    for (int wv = 0; wv < WAVES; ++wv) L = fmaf(__builtin_amdgcn_exp2f(lds[wv * REC + h] - M), lds[wv * REC + 4 + h], L);
    if (lane < 8) __builtin_amdgcn_raw_buffer_store_b32(__builtin_bit_cast(unsigned, lane < 4 ? M : L), rp, (GQ * D + lane) * 4, bid * (PREC * 4), 16);
  }
  asm volatile("s_waitcnt vmcnt(0)" ::: "memory");
  __syncthreads();
  if (tid == 0) {
    const unsigned long long old = __hip_atomic_fetch_add(p_arr, 1ull, __ATOMIC_RELAXED, __HIP_MEMORY_SCOPE_AGENT);
    const bool last = (old & 0xffffu) == NSPLIT - 1;
    if (last) __hip_atomic_store(p_epoch, tag, __ATOMIC_RELAXED, __HIP_MEMORY_SCOPE_AGENT);
    lds[WAVES * REC] = last ? 1.f : 0.f;
  }
  __syncthreads();
  if (lds[WAVES * REC] != 0.f && wave < GQ) {
    const int h = wave, half = lane >> 5;
    const unsigned rb = hk * NSPLIT * (PREC * 4);
    const float ml = __builtin_bit_cast(float, __builtin_amdgcn_raw_buffer_load_b32(rp, ((lane & 31) * PREC + GQ * D + 4 * half + h) * 4, rb, 16));
    f32x4 o[16]; float mr[16];
#pragma unroll
    for (int r = 0; r < 16; ++r) {
      mr[r] = __builtin_bit_cast(float, __builtin_amdgcn_raw_buffer_load_b32(rp, ((half * 16 + r) * PREC + GQ * D + h) * 4, rb, 16));
      o[r] = __builtin_bit_cast(f32x4, __builtin_amdgcn_raw_buffer_load_b128(rp, ((half * 16 + r) * PREC + h * D + 4 * (lane & 31)) * 4, rb, 16));
    }
    auto sw = __builtin_amdgcn_permlane32_swap(__float_as_uint(ml), __float_as_uint(ml), false, false);
    const float mi = __uint_as_float(sw[0]), li = __uint_as_float(sw[1]);
    float M = mi;
    M = fmaxf(M, dpp<XOR1>(M)); M = fmaxf(M, dpp<XOR2>(M)); M = fmaxf(M, dpp<HMIRROR>(M)); M = fmaxf(M, dpp<MIRROR>(M)); M = x16_max(M);
    float L = __builtin_amdgcn_exp2f(mi - M) * li;
    L += dpp<XOR1>(L); L += dpp<XOR2>(L); L += dpp<HMIRROR>(L); L += dpp<MIRROR>(L); L = x16_sum(L);
    f32x4 O = {0.f, 0.f, 0.f, 0.f};
#pragma unroll
    for (int r = 0; r < 16; ++r) {
      const float f = __builtin_amdgcn_exp2f(mr[r] - M);
#pragma unroll
      for (int e = 0; e < 4; ++e) O[e] = fmaf(f, o[r][e], O[e]);
    }
    const float inv = 1.f / L;
#pragma unroll
    for (int e = 0; e < 4; ++e) O[e] = x32_sum(O[e]) * inv;
    if (lane < 32) *reinterpret_cast<u32x2*>(out + (hk * GQ + h) * D + 4 * lane) = u32x2{pk_bf16(O[0], O[1]), pk_bf16(O[2], O[3])};
  }
}

}

extern "C" void kernel_launch(void* const* d_in, const int* in_sizes, int n_in, void* d_out, int out_size, void* d_ws,
                              size_t ws_size, hipStream_t stream) {
  attn_decode<<<HKV * NSPLIT, NTHR, 0, stream>>>((const unsigned short*)d_in[0], (const int*)d_in[1], (const int*)d_in[2],
                                                 (const unsigned short*)d_in[3], (const unsigned short*)d_in[4],
                                                 (const unsigned short*)d_in[5], (const unsigned short*)d_in[6],
                                                 (unsigned char*)d_ws, (unsigned short*)d_out);
}
```

```cpp
#define K_NT 1
#define V_NT 1
#include <hip/hip_runtime.h>
#include <hip/hip_bf16.h>
#include <stdint.h>

#define WAVES 8
#define NSPLIT 32

namespace {

constexpr int HQ = 32, HKV = 8, GQ = HQ / HKV, SKV = 32768, D = 128, NGK = 2, GTOK = SKV / NGK;
constexpr int NTHR = WAVES * 64;
constexpr int TOK_WG = SKV / NSPLIT;
constexpr int TOK_WAVE = TOK_WG / WAVES;
constexpr int TILE = 32;
constexpr int NTILE = TOK_WAVE / TILE;
static_assert(GQ == 4 && TOK_WAVE % TILE == 0 && GTOK % TOK_WAVE == 0, "shape");

typedef int    i32x4 __attribute__((ext_vector_type(4)));
typedef unsigned u32x4 __attribute__((ext_vector_type(4)));
typedef unsigned u32x2 __attribute__((ext_vector_type(2)));
typedef float  f32x4 __attribute__((ext_vector_type(4)));
typedef float  f32x2 __attribute__((ext_vector_type(2)));
typedef __bf16 bf16x8 __attribute__((ext_vector_type(8)));
typedef __bf16 bf16x2 __attribute__((ext_vector_type(2)));

template <int NT> __device__ __forceinline__ i32x4 ldp(const int* p) {
  if constexpr (NT) return __builtin_nontemporal_load(reinterpret_cast<const i32x4*>(p));
  else return *reinterpret_cast<const i32x4*>(p);
}
#define ldk(p) ldp<K_NT>(p)
#define ldv(p) ldp<V_NT>(p)
__device__ __forceinline__ unsigned pk_bf16(float lo, float hi) {
  f32x2 f = {lo, hi};
  bf16x2 b = __builtin_convertvector(f, bf16x2);
  return __builtin_bit_cast(unsigned, b);
}
__device__ __forceinline__ float bf_lo(unsigned u) { return __builtin_bit_cast(float, u << 16); }
__device__ __forceinline__ float bf_hi(unsigned u) { return __builtin_bit_cast(float, u & 0xffff0000u); }
template <int CTRL> __device__ __forceinline__ float dpp(float x) {
  return __builtin_bit_cast(float, __builtin_amdgcn_mov_dpp(__builtin_bit_cast(int, x), CTRL, 0xf, 0xf, true));
}
constexpr int XOR1 = 0xB1, XOR2 = 0x4E, ROR4 = 0x124, ROR8 = 0x128;
__device__ __forceinline__ float xrow_max(float x) {
  auto s = __builtin_amdgcn_permlane16_swap(__float_as_uint(x), __float_as_uint(x), false, false);
  x = fmaxf(__uint_as_float(s[0]), __uint_as_float(s[1]));
  auto t = __builtin_amdgcn_permlane32_swap(__float_as_uint(x), __float_as_uint(x), false, false);
  return fmaxf(__uint_as_float(t[0]), __uint_as_float(t[1]));
}
__device__ __forceinline__ float xrow_sum(float x) {
  auto s = __builtin_amdgcn_permlane16_swap(__float_as_uint(x), __float_as_uint(x), false, false);
  x = __uint_as_float(s[0]) + __uint_as_float(s[1]);
  auto t = __builtin_amdgcn_permlane32_swap(__float_as_uint(x), __float_as_uint(x), false, false);
  return __uint_as_float(t[0]) + __uint_as_float(t[1]);
}
__device__ __forceinline__ f32x4 mfma16(u32x4 a, u32x4 b, f32x4 c) {
  return __builtin_amdgcn_mfma_f32_16x16x32_bf16(__builtin_bit_cast(bf16x8, a), __builtin_bit_cast(bf16x8, b), c, 0, 0, 0);
}
#define SB() __builtin_amdgcn_sched_barrier(0)

constexpr int REC = 16 + GQ * D;

__global__ void __launch_bounds__(NTHR, 2) attn_split(const unsigned short* __restrict__ q, const int* __restrict__ qk,
                                                      const int* __restrict__ qv, const unsigned short* __restrict__ ks,
                                                      const unsigned short* __restrict__ kz, const unsigned short* __restrict__ vs,
                                                      const unsigned short* __restrict__ vz, float* __restrict__ po,
                                                      float* __restrict__ pml) {
  __shared__ __attribute__((aligned(16))) float lds[WAVES * REC];
  const int tid = threadIdx.x, lane = tid & 63;
  const int wave = __builtin_amdgcn_readfirstlane(tid >> 6);
  const int bid = blockIdx.x;
  const int hk = bid / NSPLIT, sp = bid % NSPLIT;
  const int tok0 = sp * TOK_WG + wave * TOK_WAVE;
  const int grp = tok0 / GTOK;
  const int g = lane >> 4;

  const int hq = (lane >> 2) & 3, cq = lane & 3;
  const int dq = 16 * g + 4 * cq;
  const bool c1 = (lane & 1) != 0, c2 = (lane & 2) != 0;
  u32x2 qr[2], ksr[2], kzr[2];
#pragma unroll
  for (int h = 0; h < 2; ++h) {
    qr[h]  = *reinterpret_cast<const u32x2*>(q + (hk * GQ + hq) * D + 64 * h + dq);
    ksr[h] = *reinterpret_cast<const u32x2*>(ks + (hk * NGK + grp) * D + 64 * h + dq);
    kzr[h] = *reinterpret_cast<const u32x2*>(kz + (hk * NGK + grp) * D + 64 * h + dq);
  }
  const unsigned short* vsp = vs + (size_t)hk * SKV + tok0 + 8 * g;
  const unsigned short* vzp = vz + (size_t)hk * SKV + tok0 + 8 * g;
  u32x4 vsn = *reinterpret_cast<const u32x4*>(vsp);
  u32x4 vzn = *reinterpret_cast<const u32x4*>(vzp);
  SB();
  const int* kp = qk + ((size_t)hk * SKV + tok0 + 8 * hq) * D + dq;
  const int* vp = qv + ((size_t)hk * SKV + tok0 + 8 * g) * D + 4 * (lane & 15);
  i32x4 K[16], V[16];
#pragma unroll
  for (int j = 0; j < 8; ++j) { K[2 * j] = ldk(kp + j * D); K[2 * j + 1] = ldk(kp + j * D + 64); }
  SB();
#pragma unroll
  for (int j = 0; j < 8; ++j) { V[2 * j] = ldv(vp + j * D); V[2 * j + 1] = ldv(vp + j * D + 64); }
  SB();

  constexpr float C2 = 0.088388347648318440f * 1.4426950408889634f;
  u32x4 Qh, Ql; float bias = 0.f;
#pragma unroll
  for (int h = 0; h < 2; ++h)
#pragma unroll
    for (int w = 0; w < 2; ++w) {
      const float a0 = bf_lo(qr[h][w]) * bf_lo(ksr[h][w]) * C2, a1 = bf_hi(qr[h][w]) * bf_hi(ksr[h][w]) * C2;
      const unsigned hi = pk_bf16(a0, a1);
      const unsigned lo = pk_bf16(a0 - bf_lo(hi), a1 - bf_hi(hi));
      Qh[2 * h + w] = hi; Ql[2 * h + w] = lo;
      bias = fmaf(-(bf_lo(hi) + bf_lo(lo)), bf_lo(kzr[h][w]), bias);
      bias = fmaf(-(bf_hi(hi) + bf_hi(lo)), bf_hi(kzr[h][w]), bias);
    }
  bias += dpp<XOR1>(bias); bias += dpp<XOR2>(bias); bias = xrow_sum(bias);

  float m = -INFINITY, lsum = 0.f, zsum = 0.f;
  f32x4 acc[8];
#pragma unroll
  for (int i = 0; i < 8; ++i) acc[i] = f32x4{0.f, 0.f, 0.f, 0.f};

  for (int it = 0; it < NTILE; ++it) {
    const bool more = it + 1 < NTILE;
    const int nxt = more ? (it + 1) * TILE : it * TILE;
    const u32x4 vsc = vsn, vzc = vzn;
    float s[8];
#pragma unroll
    for (int j = 0; j < 8; ++j) {
      u32x4 a;
      a[0] = pk_bf16((float)K[2 * j][0], (float)K[2 * j][1]);         a[1] = pk_bf16((float)K[2 * j][2], (float)K[2 * j][3]);
      a[2] = pk_bf16((float)K[2 * j + 1][0], (float)K[2 * j + 1][1]); a[3] = pk_bf16((float)K[2 * j + 1][2], (float)K[2 * j + 1][3]);
      if (more) { K[2 * j] = ldk(kp + (size_t)nxt * D + j * D); K[2 * j + 1] = ldk(kp + (size_t)nxt * D + j * D + 64); }
      f32x4 d = mfma16(a, Qh, f32x4{0.f, 0.f, 0.f, 0.f});
      d = mfma16(a, Ql, d);
      const float x0 = c1 ? d[1] : d[0], x1 = c1 ? d[3] : d[2];
      float x = c2 ? x1 : x0;
      x += dpp<XOR1>(x); x += dpp<XOR2>(x);
      s[j] = x;
    }
    if (more) { vsn = *reinterpret_cast<const u32x4*>(vsp + nxt); vzn = *reinterpret_cast<const u32x4*>(vzp + nxt); }
    float tm = fmaxf(fmaxf(fmaxf(s[0], s[1]), fmaxf(s[2], s[3])), fmaxf(fmaxf(s[4], s[5]), fmaxf(s[6], s[7])));
    tm = xrow_max(tm);
    const float mn = fmaxf(m, tm);
    const float alpha = __builtin_amdgcn_exp2f(m - mn);
    m = mn;
    float w[8];
    float ls = 0.f, zs = 0.f;
#pragma unroll
    for (int j = 0; j < 8; ++j) {
      const float p = __builtin_amdgcn_exp2f(s[j] - mn);
      const float sc = (j & 1) ? bf_hi(vsc[j >> 1]) : bf_lo(vsc[j >> 1]);
      const float zz = (j & 1) ? bf_hi(vzc[j >> 1]) : bf_lo(vzc[j >> 1]);
      w[j] = p * sc; ls += p; zs = fmaf(w[j], zz, zs);
    }
    lsum = fmaf(lsum, alpha, ls); zsum = fmaf(zsum, alpha, zs);
    u32x4 P;
#pragma unroll
    for (int j = 0; j < 4; ++j) {
      const unsigned hi = pk_bf16(w[2 * j], w[2 * j + 1]);
      const unsigned lo = pk_bf16(w[2 * j] - bf_lo(hi), w[2 * j + 1] - bf_hi(hi));
      P[j] = c1 ? lo : hi;
    }
    {
      int ai = __builtin_bit_cast(int, alpha), ag = ai;
      ag = __builtin_amdgcn_update_dpp(ag, ai, 0x150, 0x1, 0xf, false);
      ag = __builtin_amdgcn_update_dpp(ag, ai, 0x154, 0x2, 0xf, false);
      ag = __builtin_amdgcn_update_dpp(ag, ai, 0x158, 0x4, 0xf, false);
      ag = __builtin_amdgcn_update_dpp(ag, ai, 0x15C, 0x8, 0xf, false);
      const float af = __builtin_bit_cast(float, ag);
#pragma unroll
      for (int i = 0; i < 8; ++i) { acc[i][0] *= af; acc[i][1] *= af; }
    }
    u32x4 B[8];
#pragma unroll
    for (int jp = 0; jp < 4; ++jp) {
#pragma unroll
      for (int h = 0; h < 2; ++h)
#pragma unroll
        for (int e = 0; e < 4; ++e) B[4 * h + e][jp] = pk_bf16((float)V[4 * jp + h][e], (float)V[4 * jp + 2 + h][e]);
      if (more) {
#pragma unroll
        for (int u = 0; u < 2; ++u) { V[4 * jp + 2 * u] = ldv(vp + (size_t)nxt * D + (2 * jp + u) * D); V[4 * jp + 2 * u + 1] = ldv(vp + (size_t)nxt * D + (2 * jp + u) * D + 64); }
      }
    }
#pragma unroll
    for (int i = 0; i < 8; ++i) acc[i] = mfma16(P, B[i], acc[i]);
  }

  lsum = xrow_sum(lsum); zsum = xrow_sum(zsum);
  float* rec = lds + wave * REC;
  if (lane < 16 && cq == 0) { rec[hq] = m + bias; rec[4 + hq] = lsum; rec[8 + hq] = zsum; }
#pragma unroll
  for (int h = 0; h < 2; ++h) {
    f32x4 o;
#pragma unroll
    for (int e = 0; e < 4; ++e) o[e] = acc[4 * h + e][0] + acc[4 * h + e][1];
    *reinterpret_cast<f32x4*>(rec + 16 + g * D + 64 * h + 4 * (lane & 15)) = o;
  }
  __syncthreads();
  for (int o = tid; o < GQ * D; o += NTHR) {
    const int h = o >> 7, d = o & 127;
    float M = -INFINITY;
#pragma unroll
    for (int wv = 0; wv < WAVES; ++wv) M = fmaxf(M, lds[wv * REC + h]);
    float L = 0.f, O = 0.f;
#pragma unroll
    for (int wv = 0; wv < WAVES; ++wv) {
      const float f = __builtin_amdgcn_exp2f(lds[wv * REC + h] - M);
      L = fmaf(f, lds[wv * REC + 4 + h], L);
      O = fmaf(f, lds[wv * REC + 16 + o] - lds[wv * REC + 8 + h], O);
    }
    const int row = (hk * GQ + h) * NSPLIT + sp;
    po[(size_t)row * D + d] = O;
    if (d == 0) { pml[2 * row] = M; pml[2 * row + 1] = L; }
  }
}

__global__ void __launch_bounds__(128) attn_combine(const float* __restrict__ po, const float* __restrict__ pml,
                                                    __hip_bfloat16* __restrict__ out) {
  const int h = blockIdx.x, d = threadIdx.x, lane = threadIdx.x & 63;
  float o[NSPLIT];
  f32x2 ml[(NSPLIT + 63) / 64];
#pragma unroll
  for (int i = 0; i < (NSPLIT + 63) / 64; ++i) {
    const int s = lane + 64 * i;
    ml[i] = s < NSPLIT ? *reinterpret_cast<const f32x2*>(pml + 2 * (h * NSPLIT + s)) : f32x2{-INFINITY, 0.f};
  }
#pragma unroll
  for (int s = 0; s < NSPLIT; ++s) o[s] = po[(size_t)(h * NSPLIT + s) * D + d];
  float M = -INFINITY;
#pragma unroll
  for (int i = 0; i < (NSPLIT + 63) / 64; ++i) M = fmaxf(M, ml[i][0]);
  M = fmaxf(M, dpp<XOR1>(M)); M = fmaxf(M, dpp<XOR2>(M)); M = fmaxf(M, dpp<ROR4>(M)); M = fmaxf(M, dpp<ROR8>(M)); M = xrow_max(M);
  float f[(NSPLIT + 63) / 64], L = 0.f;
#pragma unroll
  for (int i = 0; i < (NSPLIT + 63) / 64; ++i) { f[i] = __builtin_amdgcn_exp2f(ml[i][0] - M); L = fmaf(f[i], ml[i][1], L); }
  L += dpp<XOR1>(L); L += dpp<XOR2>(L); L += dpp<0x141>(L); L += dpp<0x140>(L); L = xrow_sum(L);
  float O = 0.f;
#pragma unroll
  for (int s = 0; s < NSPLIT; ++s) O = fmaf(__builtin_bit_cast(float, __builtin_amdgcn_readlane(__builtin_bit_cast(int, f[s / 64]), s & 63)), o[s], O);
  out[h * D + d] = __float2bfloat16(O / L);
}

}

extern "C" void kernel_launch(void* const* d_in, const int* in_sizes, int n_in, void* d_out, int out_size, void* d_ws,
                              size_t ws_size, hipStream_t stream) {
  const unsigned short* q  = (const unsigned short*)d_in[0];
  const int* qk            = (const int*)d_in[1];
  const int* qv            = (const int*)d_in[2];
  const unsigned short* ks = (const unsigned short*)d_in[3];
  const unsigned short* kz = (const unsigned short*)d_in[4];
  const unsigned short* vs = (const unsigned short*)d_in[5];
  const unsigned short* vz = (const unsigned short*)d_in[6];
  float* po  = (float*)d_ws;
  float* pml = po + (size_t)HQ * NSPLIT * D;
  attn_split<<<HKV * NSPLIT, NTHR, 0, stream>>>(q, qk, qv, ks, kz, vs, vz, po, pml);
  attn_combine<<<HQ, 128, 0, stream>>>(po, pml, (__hip_bfloat16*)d_out);
}
```

```cpp
#include <hip/hip_runtime.h>
#include <hip/hip_bf16.h>
#include <stdint.h>
#include <stdio.h>

#define WAVES 8
#define STAMP_OFF (64u << 20)
#define STAMP_N (HKV * NSPLIT * WAVES)

namespace {

constexpr int HQ = 32, HKV = 8, GQ = HQ / HKV, SKV = 32768, D = 128, NGK = 2, GTOK = SKV / NGK;
constexpr int NSPLIT = 32;
constexpr int NTHR = WAVES * 64;
constexpr int TOK_WG = SKV / NSPLIT;
constexpr int TILE = 32;
constexpr int NTILE = TOK_WG / (WAVES * TILE);
static_assert(GQ == 4 && TOK_WG % (WAVES * TILE) == 0 && GTOK % TOK_WG == 0 && WAVES >= 4, "shape");

constexpr int CTL_BYTES = 4096;
constexpr int PREC = GQ * D + 8;
constexpr unsigned long long MASK48 = 0xFFFFFFFFFFFFull;

typedef unsigned u32x4 __attribute__((ext_vector_type(4)));
typedef unsigned u32x2 __attribute__((ext_vector_type(2)));
typedef float  f32x4 __attribute__((ext_vector_type(4)));
typedef float  f32x2 __attribute__((ext_vector_type(2)));
typedef __bf16 bf16x8 __attribute__((ext_vector_type(8)));
typedef __bf16 bf16x2 __attribute__((ext_vector_type(2)));
typedef __attribute__((address_space(1))) unsigned long long gu64;

__device__ __forceinline__ unsigned pk_bf16(float lo, float hi) {
  f32x2 f = {lo, hi};
  bf16x2 b = __builtin_convertvector(f, bf16x2);
  return __builtin_bit_cast(unsigned, b);
}
__device__ __forceinline__ unsigned pk_int(unsigned lo, unsigned hi) { return pk_bf16((float)(int)lo, (float)(int)hi); }
__device__ __forceinline__ float bf_lo(unsigned u) { return __builtin_bit_cast(float, u << 16); }
__device__ __forceinline__ float bf_hi(unsigned u) { return __builtin_bit_cast(float, u & 0xffff0000u); }
template <int CTRL> __device__ __forceinline__ float dpp(float x) {
  return __builtin_bit_cast(float, __builtin_amdgcn_mov_dpp(__builtin_bit_cast(int, x), CTRL, 0xf, 0xf, true));
}
constexpr int XOR1 = 0xB1, XOR2 = 0x4E, HMIRROR = 0x141, MIRROR = 0x140;
__device__ __forceinline__ float x16_max(float x) {
  auto s = __builtin_amdgcn_permlane16_swap(__float_as_uint(x), __float_as_uint(x), false, false);
  return fmaxf(__uint_as_float(s[0]), __uint_as_float(s[1]));
}
__device__ __forceinline__ float x32_max(float x) {
  auto t = __builtin_amdgcn_permlane32_swap(__float_as_uint(x), __float_as_uint(x), false, false);
  return fmaxf(__uint_as_float(t[0]), __uint_as_float(t[1]));
}
__device__ __forceinline__ float x16_sum(float x) {
  auto s = __builtin_amdgcn_permlane16_swap(__float_as_uint(x), __float_as_uint(x), false, false);
  return __uint_as_float(s[0]) + __uint_as_float(s[1]);
}
__device__ __forceinline__ float x32_sum(float x) {
  auto t = __builtin_amdgcn_permlane32_swap(__float_as_uint(x), __float_as_uint(x), false, false);
  return __uint_as_float(t[0]) + __uint_as_float(t[1]);
}
__device__ __forceinline__ unsigned rfl(unsigned x) { return (unsigned)__builtin_amdgcn_readfirstlane((int)x); }
__device__ __forceinline__ f32x4 mfma16(u32x4 a, u32x4 b, f32x4 c) {
  return __builtin_amdgcn_mfma_f32_16x16x32_bf16(__builtin_bit_cast(bf16x8, a), __builtin_bit_cast(bf16x8, b), c, 0, 0, 0);
}
__device__ __forceinline__ u32x4 make_srd(const void* p, unsigned bytes) {
  return u32x4{(unsigned)(size_t)p, (unsigned)((size_t)p >> 32) & 0xffffu, bytes, 0x00020000u};
}
template <int OFF> __device__ __forceinline__ void ld_nt(u32x4& dst, unsigned voff, u32x4 srd, unsigned soff) {
  asm volatile("buffer_load_dwordx4 %0, %1, %2, %3 offen offset:%4 nt" : "=v"(dst) : "v"(voff), "s"(srd), "s"(soff), "i"(OFF));
}
__device__ __forceinline__ void ld_x4(u32x4& dst, unsigned voff, u32x4 srd, unsigned soff) {
  asm volatile("buffer_load_dwordx4 %0, %1, %2, %3 offen" : "=v"(dst) : "v"(voff), "s"(srd), "s"(soff));
}
template <int OFF> __device__ __forceinline__ void ld_x2(u32x2& dst, unsigned voff, u32x4 srd, unsigned soff) {
  asm volatile("buffer_load_dwordx2 %0, %1, %2, %3 offen offset:%4" : "=v"(dst) : "v"(voff), "s"(srd), "s"(soff), "i"(OFF));
}
#define WAIT2(N, a, b) asm volatile("s_waitcnt vmcnt(" #N ")" : "+v"(a), "+v"(b))
#define WAIT4(N, a, b, c, d) asm volatile("s_waitcnt vmcnt(" #N ")" : "+v"(a), "+v"(b), "+v"(c), "+v"(d))
#define PIN(x) asm volatile("" : "+v"(x))

constexpr int REC = 16 + GQ * D;

__global__ void __launch_bounds__(NTHR, WAVES / 4) attn_decode(const unsigned short* __restrict__ q, const int* __restrict__ qk,
                                                               const int* __restrict__ qv, const unsigned short* __restrict__ ks,
                                                               const unsigned short* __restrict__ kz, const unsigned short* __restrict__ vs,
                                                               const unsigned short* __restrict__ vz, unsigned char* __restrict__ ws,
                                                               unsigned short* __restrict__ out) {
  __shared__ __attribute__((aligned(16))) float lds[WAVES * REC + 4];
  const int tid = threadIdx.x, lane = tid & 63;
  const int wave = __builtin_amdgcn_readfirstlane(tid >> 6);
  const int bid = blockIdx.x;
  const int hk = bid / NSPLIT, sp = bid % NSPLIT;
  const int grp = sp * TOK_WG / GTOK;
  const int g = lane >> 4, hq = (lane >> 2) & 3, cq = lane & 3;
  const bool c1 = (lane & 1) != 0, c2 = (lane & 2) != 0;

  u32x2 qr0, qr1, ks0, ks1, kz0, kz1;
  u32x4 ctl;
  {
    const unsigned dq2 = (16 * g + 4 * cq) * 2;
    const u32x4 sq = make_srd(q, HQ * D * 2), sks = make_srd(ks, HKV * NGK * D * 2), skz = make_srd(kz, HKV * NGK * D * 2);
    const unsigned zero = 0;
    asm volatile("buffer_load_dwordx4 %0, %1, %2, %3 offen sc1" : "=v"(ctl) : "v"(zero), "s"(make_srd(ws, CTL_BYTES)), "s"(hk * 256));
    ld_x2<0>(qr0, dq2 + hq * (D * 2), sq, hk * (GQ * D * 2));   ld_x2<128>(qr1, dq2 + hq * (D * 2), sq, hk * (GQ * D * 2));
    ld_x2<0>(ks0, dq2, sks, (hk * NGK + grp) * (D * 2));        ld_x2<128>(ks1, dq2, sks, (hk * NGK + grp) * (D * 2));
    ld_x2<0>(kz0, dq2, skz, (hk * NGK + grp) * (D * 2));        ld_x2<128>(kz1, dq2, skz, (hk * NGK + grp) * (D * 2));
  }
  u32x4 sk = make_srd(qk, HKV * SKV * D * 4u), sv = make_srd(qv, HKV * SKV * D * 4u);
  u32x4 svs = make_srd(vs, HKV * SKV * 2u), svz = make_srd(vz, HKV * SKV * 2u);
  const unsigned kofs = hq * (8 * D * 4) + g * 64 + cq * 16, vofs = g * (8 * D * 4) + (lane & 15) * 16, sofs = g * 16;
  unsigned row = hk * SKV + sp * TOK_WG + wave * TILE;
  u32x4 K[16], V[16], S, Z;
#define KLD(j) do { ld_nt<(j) * 512>(K[2 * (j)], kofs, sk, row * (D * 4)); ld_nt<(j) * 512 + 256>(K[2 * (j) + 1], kofs, sk, row * (D * 4)); } while (0)
#define VLD(j) do { ld_nt<(j) * 512>(V[2 * (j)], vofs, sv, row * (D * 4)); ld_nt<(j) * 512 + 256>(V[2 * (j) + 1], vofs, sv, row * (D * 4)); } while (0)
  KLD(0); KLD(1); KLD(2); KLD(3); KLD(4); KLD(5); KLD(6); KLD(7);
  ld_x4(S, sofs, svs, row * 2); ld_x4(Z, sofs, svz, row * 2);
  VLD(0); VLD(1); VLD(2); VLD(3); VLD(4); VLD(5); VLD(6); VLD(7);

  asm volatile("s_waitcnt vmcnt(34)" : "+v"(ctl), "+v"(qr0), "+v"(qr1), "+v"(ks0), "+v"(ks1), "+v"(kz0), "+v"(kz1));
  const unsigned long long epoch = ((unsigned long long)rfl(ctl[1]) << 32) | rfl(ctl[0]);
  const unsigned long long arr0  = ((unsigned long long)rfl(ctl[3]) << 32) | rfl(ctl[2]);
  const unsigned long long tag = (epoch * 0x5DEECE66Dull + 0xBull) & MASK48;
  gu64* const p_epoch = (gu64*)(ws + hk * 256);
  gu64* const p_arr = p_epoch + 1;
  if (tid == 0 && (arr0 >> 16) != tag) {
    unsigned long long expect = arr0;
    __hip_atomic_compare_exchange_strong(p_arr, &expect, tag << 16, __ATOMIC_RELAXED, __ATOMIC_RELAXED, __HIP_MEMORY_SCOPE_AGENT);
  }
  constexpr float C2 = 0.088388347648318440f * 1.4426950408889634f;
  u32x4 Qh, Ql; float bias = 0.f;
  {
    const unsigned qq[4] = {qr0[0], qr0[1], qr1[0], qr1[1]}, kk[4] = {ks0[0], ks0[1], ks1[0], ks1[1]}, zz[4] = {kz0[0], kz0[1], kz1[0], kz1[1]};
#pragma unroll
    for (int w = 0; w < 4; ++w) {
      const float a0 = bf_lo(qq[w]) * bf_lo(kk[w]) * C2, a1 = bf_hi(qq[w]) * bf_hi(kk[w]) * C2;
      const unsigned hi = pk_bf16(a0, a1);
      const unsigned lo = pk_bf16(a0 - bf_lo(hi), a1 - bf_hi(hi));
      Qh[w] = hi; Ql[w] = lo;
      bias = fmaf(-(bf_lo(hi) + bf_lo(lo)), bf_lo(zz[w]), bias);
      bias = fmaf(-(bf_hi(hi) + bf_hi(lo)), bf_hi(zz[w]), bias);
    }
  }
  bias += dpp<XOR1>(bias); bias += dpp<XOR2>(bias); bias = x32_sum(x16_sum(bias));

  float m = -INFINITY, lsum = 0.f, zsum = 0.f;
  f32x4 acc[8];
#pragma unroll
  for (int i = 0; i < 8; ++i) acc[i] = f32x4{0.f, 0.f, 0.f, 0.f};

#pragma unroll 1
  for (int it = 0; it < NTILE; ++it) {
    if (it + 1 < NTILE) row += WAVES * TILE;
    else { sk[2] = 0; sv[2] = 0; svs[2] = 0; svz[2] = 0; }
    float s[8];
#define QK(j) do { \
      WAIT2(32, K[2 * (j)], K[2 * (j) + 1]); \
      u32x4 a = {pk_int(K[2 * (j)][0], K[2 * (j)][1]), pk_int(K[2 * (j)][2], K[2 * (j)][3]), \
                 pk_int(K[2 * (j) + 1][0], K[2 * (j) + 1][1]), pk_int(K[2 * (j) + 1][2], K[2 * (j) + 1][3])}; \
      PIN(a); \
      KLD(j); \
      f32x4 d = mfma16(a, Qh, f32x4{0.f, 0.f, 0.f, 0.f}); \
      d = mfma16(a, Ql, d); \
      const float x0 = c1 ? d[1] : d[0], x1 = c1 ? d[3] : d[2]; \
      float x = c2 ? x1 : x0; \
      x += dpp<XOR1>(x); x += dpp<XOR2>(x); \
      s[j] = x; } while (0)
    QK(0); QK(1); QK(2); QK(3); QK(4); QK(5); QK(6); QK(7);
    float tm = fmaxf(fmaxf(fmaxf(s[0], s[1]), fmaxf(s[2], s[3])), fmaxf(fmaxf(s[4], s[5]), fmaxf(s[6], s[7])));
    tm = x32_max(x16_max(tm));
    const float mn = fmaxf(m, tm);
    const float alpha = __builtin_amdgcn_exp2f(m - mn);
    m = mn;
    WAIT2(32, S, Z);
    float w[8];
    float ls = 0.f, zs = 0.f;
#pragma unroll
    for (int j = 0; j < 8; ++j) {
      const float p = __builtin_amdgcn_exp2f(s[j] - mn);
      const float sc = (j & 1) ? bf_hi(S[j >> 1]) : bf_lo(S[j >> 1]);
      const float zz = (j & 1) ? bf_hi(Z[j >> 1]) : bf_lo(Z[j >> 1]);
      w[j] = p * sc; ls += p; zs = fmaf(w[j], zz, zs);
    }
    lsum = fmaf(lsum, alpha, ls); zsum = fmaf(zsum, alpha, zs);
    u32x4 P;
#pragma unroll
    for (int j = 0; j < 4; ++j) {
      const unsigned hi = pk_bf16(w[2 * j], w[2 * j + 1]);
      const unsigned lo = pk_bf16(w[2 * j] - bf_lo(hi), w[2 * j + 1] - bf_hi(hi));
      P[j] = c1 ? lo : hi;
    }
    asm volatile("" : "+v"(P), "+v"(zsum));
    ld_x4(S, sofs, svs, row * 2); ld_x4(Z, sofs, svz, row * 2);
    {
      int ai = __builtin_bit_cast(int, alpha), ag = ai;
      ag = __builtin_amdgcn_update_dpp(ag, ai, 0x150, 0x1, 0xf, false);
      ag = __builtin_amdgcn_update_dpp(ag, ai, 0x154, 0x2, 0xf, false);
      ag = __builtin_amdgcn_update_dpp(ag, ai, 0x158, 0x4, 0xf, false);
      ag = __builtin_amdgcn_update_dpp(ag, ai, 0x15C, 0x8, 0xf, false);
      const float af = __builtin_bit_cast(float, ag);
#pragma unroll
      for (int i = 0; i < 8; ++i) { acc[i][0] *= af; acc[i][1] *= af; }
    }
    u32x4 B[8];
#define PVC(jp) do { \
      WAIT4(30, V[4 * (jp)], V[4 * (jp) + 1], V[4 * (jp) + 2], V[4 * (jp) + 3]); \
      unsigned b0 = pk_int(V[4 * (jp)][0], V[4 * (jp) + 2][0]), b1 = pk_int(V[4 * (jp)][1], V[4 * (jp) + 2][1]), \
               b2 = pk_int(V[4 * (jp)][2], V[4 * (jp) + 2][2]), b3 = pk_int(V[4 * (jp)][3], V[4 * (jp) + 2][3]), \
               b4 = pk_int(V[4 * (jp) + 1][0], V[4 * (jp) + 3][0]), b5 = pk_int(V[4 * (jp) + 1][1], V[4 * (jp) + 3][1]), \
               b6 = pk_int(V[4 * (jp) + 1][2], V[4 * (jp) + 3][2]), b7 = pk_int(V[4 * (jp) + 1][3], V[4 * (jp) + 3][3]); \
      asm volatile("" : "+v"(b0), "+v"(b1), "+v"(b2), "+v"(b3), "+v"(b4), "+v"(b5), "+v"(b6), "+v"(b7)); \
      VLD(2 * (jp)); VLD(2 * (jp) + 1); \
      B[0][jp] = b0; B[1][jp] = b1; B[2][jp] = b2; B[3][jp] = b3; B[4][jp] = b4; B[5][jp] = b5; B[6][jp] = b6; B[7][jp] = b7; } while (0)
    PVC(0); PVC(1); PVC(2); PVC(3);
#pragma unroll
    for (int i = 0; i < 8; ++i) acc[i] = mfma16(P, B[i], acc[i]);
  }
  asm volatile("s_waitcnt vmcnt(0)" : "+v"(K[0]), "+v"(K[1]), "+v"(K[2]), "+v"(K[3]), "+v"(K[4]), "+v"(K[5]), "+v"(K[6]), "+v"(K[7]),
               "+v"(K[8]), "+v"(K[9]), "+v"(K[10]), "+v"(K[11]), "+v"(K[12]), "+v"(K[13]), "+v"(K[14]), "+v"(K[15]), "+v"(S), "+v"(Z));
  asm volatile("" : "+v"(V[0]), "+v"(V[1]), "+v"(V[2]), "+v"(V[3]), "+v"(V[4]), "+v"(V[5]), "+v"(V[6]), "+v"(V[7]),
               "+v"(V[8]), "+v"(V[9]), "+v"(V[10]), "+v"(V[11]), "+v"(V[12]), "+v"(V[13]), "+v"(V[14]), "+v"(V[15]));

  lsum = x32_sum(x16_sum(lsum)); zsum = x32_sum(x16_sum(zsum));
  float* rec = lds + wave * REC;
  if (lane < 16 && cq == 0) { rec[hq] = m + bias; rec[4 + hq] = lsum; rec[8 + hq] = zsum; }
#pragma unroll
  for (int h = 0; h < 2; ++h) {
    f32x4 o;
#pragma unroll
    for (int e = 0; e < 4; ++e) o[e] = acc[4 * h + e][0] + acc[4 * h + e][1];
    *reinterpret_cast<f32x4*>(rec + 16 + g * D + 64 * h + 4 * (lane & 15)) = o;
  }
  __syncthreads();
  const __amdgpu_buffer_rsrc_t rp = __builtin_amdgcn_make_buffer_rsrc(ws + CTL_BYTES, 0, HKV * NSPLIT * PREC * 4, 0x00020000);
  if (wave < 2) {
    const int h = tid >> 5;
    float M = -INFINITY;
#pragma unroll
    for (int wv = 0; wv < WAVES; ++wv) M = fmaxf(M, lds[wv * REC + h]);
    f32x4 O = {0.f, 0.f, 0.f, 0.f};
#pragma unroll
    for (int wv = 0; wv < WAVES; ++wv) {
      const float f = __builtin_amdgcn_exp2f(lds[wv * REC + h] - M), fz = -f * lds[wv * REC + 8 + h];
      const f32x4 o = *reinterpret_cast<const f32x4*>(lds + wv * REC + 16 + 4 * tid);
#pragma unroll
      for (int e = 0; e < 4; ++e) O[e] += fmaf(f, o[e], fz);
    }
    __builtin_amdgcn_raw_buffer_store_b128(__builtin_bit_cast(u32x4, O), rp, tid * 16, bid * (PREC * 4), 16);
  } else if (wave == 2) {
    const int h = lane & 3;
    float M = -INFINITY;
#pragma unroll
    for (int wv = 0; wv < WAVES; ++wv) M = fmaxf(M, lds[wv * REC + h]);
    float L = 0.f;
#pragma unroll
    for (int wv = 0; wv < WAVES; ++wv) L = fmaf(__builtin_amdgcn_exp2f(lds[wv * REC + h] - M), lds[wv * REC + 4 + h], L);
    if (lane < 8) __builtin_amdgcn_raw_buffer_store_b32(__builtin_bit_cast(unsigned, lane < 4 ? M : L), rp, (GQ * D + lane) * 4, bid * (PREC * 4), 16);
  }
  asm volatile("s_waitcnt vmcnt(0)" ::: "memory");
  __syncthreads();
  if (tid == 0) {
    const unsigned long long old = __hip_atomic_fetch_add(p_arr, 1ull, __ATOMIC_RELAXED, __HIP_MEMORY_SCOPE_AGENT);
    const bool last = (old & 0xffffu) == NSPLIT - 1;
    if (last) __hip_atomic_store(p_epoch, tag, __ATOMIC_RELAXED, __HIP_MEMORY_SCOPE_AGENT);
    lds[WAVES * REC] = last ? 1.f : 0.f;
  }
  __syncthreads();
  if (lds[WAVES * REC] != 0.f && wave < GQ) {
    const int h = wave, half = lane >> 5;
    const unsigned rb = hk * NSPLIT * (PREC * 4);
    const float ml = __builtin_bit_cast(float, __builtin_amdgcn_raw_buffer_load_b32(rp, ((lane & 31) * PREC + GQ * D + 4 * half + h) * 4, rb, 16));
    f32x4 o[16]; float mr[16];
#pragma unroll
    for (int r = 0; r < 16; ++r) {
      mr[r] = __builtin_bit_cast(float, __builtin_amdgcn_raw_buffer_load_b32(rp, ((half * 16 + r) * PREC + GQ * D + h) * 4, rb, 16));
      o[r] = __builtin_bit_cast(f32x4, __builtin_amdgcn_raw_buffer_load_b128(rp, ((half * 16 + r) * PREC + h * D + 4 * (lane & 31)) * 4, rb, 16));
    }
    auto sw = __builtin_amdgcn_permlane32_swap(__float_as_uint(ml), __float_as_uint(ml), false, false);
    const float mi = __uint_as_float(sw[0]), li = __uint_as_float(sw[1]);
    float M = mi;
    M = fmaxf(M, dpp<XOR1>(M)); M = fmaxf(M, dpp<XOR2>(M)); M = fmaxf(M, dpp<HMIRROR>(M)); M = fmaxf(M, dpp<MIRROR>(M)); M = x16_max(M);
    float L = __builtin_amdgcn_exp2f(mi - M) * li;
    L += dpp<XOR1>(L); L += dpp<XOR2>(L); L += dpp<HMIRROR>(L); L += dpp<MIRROR>(L); L = x16_sum(L);
    f32x4 O = {0.f, 0.f, 0.f, 0.f};
#pragma unroll
    for (int r = 0; r < 16; ++r) {
      const float f = __builtin_amdgcn_exp2f(mr[r] - M);
#pragma unroll
      for (int e = 0; e < 4; ++e) O[e] = fmaf(f, o[r][e], O[e]);
    }
    const float inv = 1.f / L;
#pragma unroll
    for (int e = 0; e < 4; ++e) O[e] = x32_sum(O[e]) * inv;
    if (lane < 32) *reinterpret_cast<u32x2*>(out + (hk * GQ + h) * D + 4 * lane) = u32x2{pk_bf16(O[0], O[1]), pk_bf16(O[2], O[3])};
  }
}

}

extern "C" void kernel_launch(void* const* d_in, const int* in_sizes, int n_in, void* d_out, int out_size, void* d_ws,
                              size_t ws_size, hipStream_t stream) {
  fprintf(stderr, "ptrs q %p qk %p qv %p ks %p kz %p vs %p vz %p out %p ws %p\n", d_in[0], d_in[1], d_in[2], d_in[3], d_in[4], d_in[5], d_in[6], d_out, d_ws);
  attn_decode<<<HKV * NSPLIT, NTHR, 0, stream>>>((const unsigned short*)d_in[0], (const int*)d_in[1], (const int*)d_in[2],
                                                 (const unsigned short*)d_in[3], (const unsigned short*)d_in[4],
                                                 (const unsigned short*)d_in[5], (const unsigned short*)d_in[6],
                                                 (unsigned char*)d_ws, (unsigned short*)d_out);
}
```

```cpp
#define WAVES 4
#include <hip/hip_runtime.h>
#include <hip/hip_bf16.h>
#include <stdint.h>

#define STAMP_OFF (64u << 20)
#define STAMP_N (HKV * NSPLIT * WAVES)

namespace {

constexpr int HQ = 32, HKV = 8, GQ = HQ / HKV, SKV = 32768, D = 128, NGK = 2, GTOK = SKV / NGK;
constexpr int NSPLIT = 32;
constexpr int NTHR = WAVES * 64;
constexpr int TOK_WG = SKV / NSPLIT;
constexpr int TILE = 32;
constexpr int NTILE = TOK_WG / (WAVES * TILE);
static_assert(GQ == 4 && TOK_WG % (WAVES * TILE) == 0 && GTOK % TOK_WG == 0 && WAVES >= 4, "shape");

constexpr int CTL_BYTES = 4096;
constexpr int PREC = GQ * D + 8;
constexpr unsigned long long MASK48 = 0xFFFFFFFFFFFFull;

typedef unsigned u32x4 __attribute__((ext_vector_type(4)));
typedef unsigned u32x2 __attribute__((ext_vector_type(2)));
typedef float  f32x4 __attribute__((ext_vector_type(4)));
typedef float  f32x2 __attribute__((ext_vector_type(2)));
typedef __bf16 bf16x8 __attribute__((ext_vector_type(8)));
typedef __bf16 bf16x2 __attribute__((ext_vector_type(2)));
typedef __attribute__((address_space(1))) unsigned long long gu64;

__device__ __forceinline__ unsigned pk_bf16(float lo, float hi) {
  f32x2 f = {lo, hi};
  bf16x2 b = __builtin_convertvector(f, bf16x2);
  return __builtin_bit_cast(unsigned, b);
}
__device__ __forceinline__ unsigned pk_int(unsigned lo, unsigned hi) { return pk_bf16((float)(int)lo, (float)(int)hi); }
__device__ __forceinline__ float bf_lo(unsigned u) { return __builtin_bit_cast(float, u << 16); }
__device__ __forceinline__ float bf_hi(unsigned u) { return __builtin_bit_cast(float, u & 0xffff0000u); }
template <int CTRL> __device__ __forceinline__ float dpp(float x) {
  return __builtin_bit_cast(float, __builtin_amdgcn_mov_dpp(__builtin_bit_cast(int, x), CTRL, 0xf, 0xf, true));
}
constexpr int XOR1 = 0xB1, XOR2 = 0x4E, HMIRROR = 0x141, MIRROR = 0x140;
__device__ __forceinline__ float x16_max(float x) {
  auto s = __builtin_amdgcn_permlane16_swap(__float_as_uint(x), __float_as_uint(x), false, false);
  return fmaxf(__uint_as_float(s[0]), __uint_as_float(s[1]));
}
__device__ __forceinline__ float x32_max(float x) {
  auto t = __builtin_amdgcn_permlane32_swap(__float_as_uint(x), __float_as_uint(x), false, false);
  return fmaxf(__uint_as_float(t[0]), __uint_as_float(t[1]));
}
__device__ __forceinline__ float x16_sum(float x) {
  auto s = __builtin_amdgcn_permlane16_swap(__float_as_uint(x), __float_as_uint(x), false, false);
  return __uint_as_float(s[0]) + __uint_as_float(s[1]);
}
__device__ __forceinline__ float x32_sum(float x) {
  auto t = __builtin_amdgcn_permlane32_swap(__float_as_uint(x), __float_as_uint(x), false, false);
  return __uint_as_float(t[0]) + __uint_as_float(t[1]);
}
__device__ __forceinline__ unsigned rfl(unsigned x) { return (unsigned)__builtin_amdgcn_readfirstlane((int)x); }
__device__ __forceinline__ f32x4 mfma16(u32x4 a, u32x4 b, f32x4 c) {
  return __builtin_amdgcn_mfma_f32_16x16x32_bf16(__builtin_bit_cast(bf16x8, a), __builtin_bit_cast(bf16x8, b), c, 0, 0, 0);
}
__device__ __forceinline__ u32x4 make_srd(const void* p, unsigned bytes) {
  return u32x4{(unsigned)(size_t)p, (unsigned)((size_t)p >> 32) & 0xffffu, bytes, 0x00020000u};
}
template <int OFF> __device__ __forceinline__ void ld_nt(u32x4& dst, unsigned voff, u32x4 srd, unsigned soff) {
  asm volatile("buffer_load_dwordx4 %0, %1, %2, %3 offen offset:%4 nt" : "=v"(dst) : "v"(voff), "s"(srd), "s"(soff), "i"(OFF));
}
__device__ __forceinline__ void ld_x4(u32x4& dst, unsigned voff, u32x4 srd, unsigned soff) {
  asm volatile("buffer_load_dwordx4 %0, %1, %2, %3 offen" : "=v"(dst) : "v"(voff), "s"(srd), "s"(soff));
}
template <int OFF> __device__ __forceinline__ void ld_x2(u32x2& dst, unsigned voff, u32x4 srd, unsigned soff) {
  asm volatile("buffer_load_dwordx2 %0, %1, %2, %3 offen offset:%4" : "=v"(dst) : "v"(voff), "s"(srd), "s"(soff), "i"(OFF));
}
#define WAIT2(N, a, b) asm volatile("s_waitcnt vmcnt(" #N ")" : "+v"(a), "+v"(b))
#define WAIT4(N, a, b, c, d) asm volatile("s_waitcnt vmcnt(" #N ")" : "+v"(a), "+v"(b), "+v"(c), "+v"(d))
#define PIN(x) asm volatile("" : "+v"(x))

constexpr int REC = 16 + GQ * D;

__global__ void __launch_bounds__(NTHR, WAVES / 4) attn_decode(const unsigned short* __restrict__ q, const int* __restrict__ qk,
                                                               const int* __restrict__ qv, const unsigned short* __restrict__ ks,
                                                               const unsigned short* __restrict__ kz, const unsigned short* __restrict__ vs,
                                                               const unsigned short* __restrict__ vz, unsigned char* __restrict__ ws,
                                                               unsigned short* __restrict__ out) {
  __shared__ __attribute__((aligned(16))) float lds[WAVES * REC + 4];
  const int tid = threadIdx.x, lane = tid & 63;
  const int wave = __builtin_amdgcn_readfirstlane(tid >> 6);
  const int bid = blockIdx.x;
  const int hk = bid / NSPLIT, sp = bid % NSPLIT;
  const int grp = sp * TOK_WG / GTOK;
  const int g = lane >> 4, hq = (lane >> 2) & 3, cq = lane & 3;
  const bool c1 = (lane & 1) != 0, c2 = (lane & 2) != 0;

  const unsigned long long d0 = __builtin_amdgcn_s_memrealtime();
  u32x2 qr0, qr1, ks0, ks1, kz0, kz1;
  u32x4 ctl;
  {
    const unsigned dq2 = (16 * g + 4 * cq) * 2;
    const u32x4 sq = make_srd(q, HQ * D * 2), sks = make_srd(ks, HKV * NGK * D * 2), skz = make_srd(kz, HKV * NGK * D * 2);
    const unsigned zero = 0;
    asm volatile("buffer_load_dwordx4 %0, %1, %2, %3 offen sc1" : "=v"(ctl) : "v"(zero), "s"(make_srd(ws, CTL_BYTES)), "s"(hk * 256));
    ld_x2<0>(qr0, dq2 + hq * (D * 2), sq, hk * (GQ * D * 2));   ld_x2<128>(qr1, dq2 + hq * (D * 2), sq, hk * (GQ * D * 2));
    ld_x2<0>(ks0, dq2, sks, (hk * NGK + grp) * (D * 2));        ld_x2<128>(ks1, dq2, sks, (hk * NGK + grp) * (D * 2));
    ld_x2<0>(kz0, dq2, skz, (hk * NGK + grp) * (D * 2));        ld_x2<128>(kz1, dq2, skz, (hk * NGK + grp) * (D * 2));
  }
  u32x4 sk = make_srd(qk, HKV * SKV * D * 4u), sv = make_srd(qv, HKV * SKV * D * 4u);
  u32x4 svs = make_srd(vs, HKV * SKV * 2u), svz = make_srd(vz, HKV * SKV * 2u);
  const unsigned kofs = hq * (8 * D * 4) + g * 64 + cq * 16, vofs = g * (8 * D * 4) + (lane & 15) * 16, sofs = g * 16;
  unsigned row = hk * SKV + sp * TOK_WG + wave * TILE;
  u32x4 K[16], V[16], S, Z;
#define KLD(j) do { ld_nt<(j) * 512>(K[2 * (j)], kofs, sk, row * (D * 4)); ld_nt<(j) * 512 + 256>(K[2 * (j) + 1], kofs, sk, row * (D * 4)); } while (0)
#define VLD(j) do { ld_nt<(j) * 512>(V[2 * (j)], vofs, sv, row * (D * 4)); ld_nt<(j) * 512 + 256>(V[2 * (j) + 1], vofs, sv, row * (D * 4)); } while (0)
  KLD(0); KLD(1); KLD(2); KLD(3); KLD(4); KLD(5); KLD(6); KLD(7);
  ld_x4(S, sofs, svs, row * 2); ld_x4(Z, sofs, svz, row * 2);
  VLD(0); VLD(1); VLD(2); VLD(3); VLD(4); VLD(5); VLD(6); VLD(7);

  asm volatile("s_waitcnt vmcnt(34)" : "+v"(ctl), "+v"(qr0), "+v"(qr1), "+v"(ks0), "+v"(ks1), "+v"(kz0), "+v"(kz1));
  const unsigned long long epoch = ((unsigned long long)rfl(ctl[1]) << 32) | rfl(ctl[0]);
  const unsigned long long arr0  = ((unsigned long long)rfl(ctl[3]) << 32) | rfl(ctl[2]);
  const unsigned long long tag = (epoch * 0x5DEECE66Dull + 0xBull) & MASK48;
  gu64* const p_epoch = (gu64*)(ws + hk * 256);
  gu64* const p_arr = p_epoch + 1;
  if (tid == 0 && (arr0 >> 16) != tag) {
    unsigned long long expect = arr0;
    __hip_atomic_compare_exchange_strong(p_arr, &expect, tag << 16, __ATOMIC_RELAXED, __ATOMIC_RELAXED, __HIP_MEMORY_SCOPE_AGENT);
  }
  constexpr float C2 = 0.088388347648318440f * 1.4426950408889634f;
  u32x4 Qh, Ql; float bias = 0.f;
  {
    const unsigned qq[4] = {qr0[0], qr0[1], qr1[0], qr1[1]}, kk[4] = {ks0[0], ks0[1], ks1[0], ks1[1]}, zz[4] = {kz0[0], kz0[1], kz1[0], kz1[1]};
#pragma unroll
    for (int w = 0; w < 4; ++w) {
      const float a0 = bf_lo(qq[w]) * bf_lo(kk[w]) * C2, a1 = bf_hi(qq[w]) * bf_hi(kk[w]) * C2;
      const unsigned hi = pk_bf16(a0, a1);
      const unsigned lo = pk_bf16(a0 - bf_lo(hi), a1 - bf_hi(hi));
      Qh[w] = hi; Ql[w] = lo;
      bias = fmaf(-(bf_lo(hi) + bf_lo(lo)), bf_lo(zz[w]), bias);
      bias = fmaf(-(bf_hi(hi) + bf_hi(lo)), bf_hi(zz[w]), bias);
    }
  }
  bias += dpp<XOR1>(bias); bias += dpp<XOR2>(bias); bias = x32_sum(x16_sum(bias));

  asm volatile("" :: "v"(bias));
  const unsigned long long d1 = __builtin_amdgcn_s_memrealtime();
  float m = -INFINITY, lsum = 0.f, zsum = 0.f;
  f32x4 acc[8];
#pragma unroll
  for (int i = 0; i < 8; ++i) acc[i] = f32x4{0.f, 0.f, 0.f, 0.f};

#pragma unroll 1
  for (int it = 0; it < NTILE; ++it) {
    if (it + 1 < NTILE) row += WAVES * TILE;
    else { sk[2] = 0; sv[2] = 0; svs[2] = 0; svz[2] = 0; }
    float s[8];
#define QK(j) do { \
      WAIT2(32, K[2 * (j)], K[2 * (j) + 1]); \
      u32x4 a = {pk_int(K[2 * (j)][0], K[2 * (j)][1]), pk_int(K[2 * (j)][2], K[2 * (j)][3]), \
                 pk_int(K[2 * (j) + 1][0], K[2 * (j) + 1][1]), pk_int(K[2 * (j) + 1][2], K[2 * (j) + 1][3])}; \
      PIN(a); \
      KLD(j); \
      f32x4 d = mfma16(a, Qh, f32x4{0.f, 0.f, 0.f, 0.f}); \
      d = mfma16(a, Ql, d); \
      const float x0 = c1 ? d[1] : d[0], x1 = c1 ? d[3] : d[2]; \
      float x = c2 ? x1 : x0; \
      x += dpp<XOR1>(x); x += dpp<XOR2>(x); \
      s[j] = x; } while (0)
    QK(0); QK(1); QK(2); QK(3); QK(4); QK(5); QK(6); QK(7);
    float tm = fmaxf(fmaxf(fmaxf(s[0], s[1]), fmaxf(s[2], s[3])), fmaxf(fmaxf(s[4], s[5]), fmaxf(s[6], s[7])));
    tm = x32_max(x16_max(tm));
    const float mn = fmaxf(m, tm);
    const float alpha = __builtin_amdgcn_exp2f(m - mn);
    m = mn;
    WAIT2(32, S, Z);
    float w[8];
    float ls = 0.f, zs = 0.f;
#pragma unroll
    for (int j = 0; j < 8; ++j) {
      const float p = __builtin_amdgcn_exp2f(s[j] - mn);
      const float sc = (j & 1) ? bf_hi(S[j >> 1]) : bf_lo(S[j >> 1]);
      const float zz = (j & 1) ? bf_hi(Z[j >> 1]) : bf_lo(Z[j >> 1]);
      w[j] = p * sc; ls += p; zs = fmaf(w[j], zz, zs);
    }
    lsum = fmaf(lsum, alpha, ls); zsum = fmaf(zsum, alpha, zs);
    u32x4 P;
#pragma unroll
    for (int j = 0; j < 4; ++j) {
      const unsigned hi = pk_bf16(w[2 * j], w[2 * j + 1]);
      const unsigned lo = pk_bf16(w[2 * j] - bf_lo(hi), w[2 * j + 1] - bf_hi(hi));
      P[j] = c1 ? lo : hi;
    }
    asm volatile("" : "+v"(P), "+v"(zsum));
    ld_x4(S, sofs, svs, row * 2); ld_x4(Z, sofs, svz, row * 2);
    {
      int ai = __builtin_bit_cast(int, alpha), ag = ai;
      ag = __builtin_amdgcn_update_dpp(ag, ai, 0x150, 0x1, 0xf, false);
      ag = __builtin_amdgcn_update_dpp(ag, ai, 0x154, 0x2, 0xf, false);
      ag = __builtin_amdgcn_update_dpp(ag, ai, 0x158, 0x4, 0xf, false);
      ag = __builtin_amdgcn_update_dpp(ag, ai, 0x15C, 0x8, 0xf, false);
      const float af = __builtin_bit_cast(float, ag);
#pragma unroll
      for (int i = 0; i < 8; ++i) { acc[i][0] *= af; acc[i][1] *= af; }
    }
    u32x4 B[8];
#define PVC(jp) do { \
      WAIT4(30, V[4 * (jp)], V[4 * (jp) + 1], V[4 * (jp) + 2], V[4 * (jp) + 3]); \
      unsigned b0 = pk_int(V[4 * (jp)][0], V[4 * (jp) + 2][0]), b1 = pk_int(V[4 * (jp)][1], V[4 * (jp) + 2][1]), \
               b2 = pk_int(V[4 * (jp)][2], V[4 * (jp) + 2][2]), b3 = pk_int(V[4 * (jp)][3], V[4 * (jp) + 2][3]), \
               b4 = pk_int(V[4 * (jp) + 1][0], V[4 * (jp) + 3][0]), b5 = pk_int(V[4 * (jp) + 1][1], V[4 * (jp) + 3][1]), \
               b6 = pk_int(V[4 * (jp) + 1][2], V[4 * (jp) + 3][2]), b7 = pk_int(V[4 * (jp) + 1][3], V[4 * (jp) + 3][3]); \
      asm volatile("" : "+v"(b0), "+v"(b1), "+v"(b2), "+v"(b3), "+v"(b4), "+v"(b5), "+v"(b6), "+v"(b7)); \
      VLD(2 * (jp)); VLD(2 * (jp) + 1); \
      B[0][jp] = b0; B[1][jp] = b1; B[2][jp] = b2; B[3][jp] = b3; B[4][jp] = b4; B[5][jp] = b5; B[6][jp] = b6; B[7][jp] = b7; } while (0)
    PVC(0); PVC(1); PVC(2); PVC(3);
#pragma unroll
    for (int i = 0; i < 8; ++i) acc[i] = mfma16(P, B[i], acc[i]);
  }
  asm volatile("s_waitcnt vmcnt(0)" : "+v"(K[0]), "+v"(K[1]), "+v"(K[2]), "+v"(K[3]), "+v"(K[4]), "+v"(K[5]), "+v"(K[6]), "+v"(K[7]),
               "+v"(K[8]), "+v"(K[9]), "+v"(K[10]), "+v"(K[11]), "+v"(K[12]), "+v"(K[13]), "+v"(K[14]), "+v"(K[15]), "+v"(S), "+v"(Z));
  asm volatile("" : "+v"(V[0]), "+v"(V[1]), "+v"(V[2]), "+v"(V[3]), "+v"(V[4]), "+v"(V[5]), "+v"(V[6]), "+v"(V[7]),
               "+v"(V[8]), "+v"(V[9]), "+v"(V[10]), "+v"(V[11]), "+v"(V[12]), "+v"(V[13]), "+v"(V[14]), "+v"(V[15]));

  asm volatile("" :: "v"(acc[0]), "v"(acc[7]));
  const unsigned long long d2 = __builtin_amdgcn_s_memrealtime();
  lsum = x32_sum(x16_sum(lsum)); zsum = x32_sum(x16_sum(zsum));
  float* rec = lds + wave * REC;
  if (lane < 16 && cq == 0) { rec[hq] = m + bias; rec[4 + hq] = lsum; rec[8 + hq] = zsum; }
#pragma unroll
  for (int h = 0; h < 2; ++h) {
    f32x4 o;
#pragma unroll
    for (int e = 0; e < 4; ++e) o[e] = acc[4 * h + e][0] + acc[4 * h + e][1];
    *reinterpret_cast<f32x4*>(rec + 16 + g * D + 64 * h + 4 * (lane & 15)) = o;
  }
  __syncthreads();
  const __amdgpu_buffer_rsrc_t rp = __builtin_amdgcn_make_buffer_rsrc(ws + CTL_BYTES, 0, HKV * NSPLIT * PREC * 4, 0x00020000);
  if (wave < 2) {
    const int h = tid >> 5;
    float M = -INFINITY;
#pragma unroll
    for (int wv = 0; wv < WAVES; ++wv) M = fmaxf(M, lds[wv * REC + h]);
    f32x4 O = {0.f, 0.f, 0.f, 0.f};
#pragma unroll
    for (int wv = 0; wv < WAVES; ++wv) {
      const float f = __builtin_amdgcn_exp2f(lds[wv * REC + h] - M), fz = -f * lds[wv * REC + 8 + h];
      const f32x4 o = *reinterpret_cast<const f32x4*>(lds + wv * REC + 16 + 4 * tid);
#pragma unroll
      for (int e = 0; e < 4; ++e) O[e] += fmaf(f, o[e], fz);
    }
    __builtin_amdgcn_raw_buffer_store_b128(__builtin_bit_cast(u32x4, O), rp, tid * 16, bid * (PREC * 4), 16);
  } else if (wave == 2) {
    const int h = lane & 3;
    float M = -INFINITY;
#pragma unroll
    for (int wv = 0; wv < WAVES; ++wv) M = fmaxf(M, lds[wv * REC + h]);
    float L = 0.f;
#pragma unroll
    for (int wv = 0; wv < WAVES; ++wv) L = fmaf(__builtin_amdgcn_exp2f(lds[wv * REC + h] - M), lds[wv * REC + 4 + h], L);
    if (lane < 8) __builtin_amdgcn_raw_buffer_store_b32(__builtin_bit_cast(unsigned, lane < 4 ? M : L), rp, (GQ * D + lane) * 4, bid * (PREC * 4), 16);
  }
  asm volatile("s_waitcnt vmcnt(0)" ::: "memory");
  __syncthreads();
  if (tid == 0) {
    const unsigned long long old = __hip_atomic_fetch_add(p_arr, 1ull, __ATOMIC_RELAXED, __HIP_MEMORY_SCOPE_AGENT);
    const bool last = (old & 0xffffu) == NSPLIT - 1;
    if (last) __hip_atomic_store(p_epoch, tag, __ATOMIC_RELAXED, __HIP_MEMORY_SCOPE_AGENT);
    lds[WAVES * REC] = last ? 1.f : 0.f;
  }
  __syncthreads();
  if (lds[WAVES * REC] != 0.f && wave < GQ) {
    const int h = wave, half = lane >> 5;
    const unsigned rb = hk * NSPLIT * (PREC * 4);
    const float ml = __builtin_bit_cast(float, __builtin_amdgcn_raw_buffer_load_b32(rp, ((lane & 31) * PREC + GQ * D + 4 * half + h) * 4, rb, 16));
    f32x4 o[16]; float mr[16];
#pragma unroll
    for (int r = 0; r < 16; ++r) {
      mr[r] = __builtin_bit_cast(float, __builtin_amdgcn_raw_buffer_load_b32(rp, ((half * 16 + r) * PREC + GQ * D + h) * 4, rb, 16));
      o[r] = __builtin_bit_cast(f32x4, __builtin_amdgcn_raw_buffer_load_b128(rp, ((half * 16 + r) * PREC + h * D + 4 * (lane & 31)) * 4, rb, 16));
    }
    auto sw = __builtin_amdgcn_permlane32_swap(__float_as_uint(ml), __float_as_uint(ml), false, false);
    const float mi = __uint_as_float(sw[0]), li = __uint_as_float(sw[1]);
    float M = mi;
    M = fmaxf(M, dpp<XOR1>(M)); M = fmaxf(M, dpp<XOR2>(M)); M = fmaxf(M, dpp<HMIRROR>(M)); M = fmaxf(M, dpp<MIRROR>(M)); M = x16_max(M);
    float L = __builtin_amdgcn_exp2f(mi - M) * li;
    L += dpp<XOR1>(L); L += dpp<XOR2>(L); L += dpp<HMIRROR>(L); L += dpp<MIRROR>(L); L = x16_sum(L);
    f32x4 O = {0.f, 0.f, 0.f, 0.f};
#pragma unroll
    for (int r = 0; r < 16; ++r) {
      const float f = __builtin_amdgcn_exp2f(mr[r] - M);
#pragma unroll
      for (int e = 0; e < 4; ++e) O[e] = fmaf(f, o[r][e], O[e]);
    }
    const float inv = 1.f / L;
#pragma unroll
    for (int e = 0; e < 4; ++e) O[e] = x32_sum(O[e]) * inv;
    if (lane < 32) *reinterpret_cast<u32x2*>(out + (hk * GQ + h) * D + 4 * lane) = u32x2{pk_bf16(O[0], O[1]), pk_bf16(O[2], O[3])};
    if (wave == 0) { asm volatile("s_waitcnt vmcnt(0)" ::: "memory"); const unsigned long long d3 = __builtin_amdgcn_s_memrealtime();
      const unsigned long long until = d3 + (0ull);
      while (__builtin_amdgcn_s_memrealtime() < until) __builtin_amdgcn_s_sleep(1); }
  }
}

}

extern "C" void kernel_launch(void* const* d_in, const int* in_sizes, int n_in, void* d_out, int out_size, void* d_ws,
                              size_t ws_size, hipStream_t stream) {
  attn_decode<<<HKV * NSPLIT, NTHR, 0, stream>>>((const unsigned short*)d_in[0], (const int*)d_in[1], (const int*)d_in[2],
                                                 (const unsigned short*)d_in[3], (const unsigned short*)d_in[4],
                                                 (const unsigned short*)d_in[5], (const unsigned short*)d_in[6],
                                                 (unsigned char*)d_ws, (unsigned short*)d_out);
}
```

```cpp
#include <hip/hip_runtime.h>
#include <hip/hip_bf16.h>
#include <stdint.h>

#define WAVES 8
#define MAPV 1
#define STAMP_OFF (64u << 20)
#define STAMP_N (HKV * NSPLIT * WAVES)

namespace {

constexpr int HQ = 32, HKV = 8, GQ = HQ / HKV, SKV = 32768, D = 128, NGK = 2, GTOK = SKV / NGK;
constexpr int NSPLIT = 32;
constexpr int NTHR = WAVES * 64;
constexpr int TOK_WG = SKV / NSPLIT;
constexpr int TILE = 32;
constexpr int NTILE = TOK_WG / (WAVES * TILE);
static_assert(GQ == 4 && TOK_WG % (WAVES * TILE) == 0 && GTOK % TOK_WG == 0 && WAVES >= 4, "shape");

constexpr int CTL_BYTES = 4096;
constexpr int PREC = GQ * D + 8;
constexpr unsigned long long MASK48 = 0xFFFFFFFFFFFFull;

typedef unsigned u32x4 __attribute__((ext_vector_type(4)));
typedef unsigned u32x2 __attribute__((ext_vector_type(2)));
typedef float  f32x4 __attribute__((ext_vector_type(4)));
typedef float  f32x2 __attribute__((ext_vector_type(2)));
typedef __bf16 bf16x8 __attribute__((ext_vector_type(8)));
typedef __bf16 bf16x2 __attribute__((ext_vector_type(2)));
typedef __attribute__((address_space(1))) unsigned long long gu64;

__device__ __forceinline__ unsigned pk_bf16(float lo, float hi) {
  f32x2 f = {lo, hi};
  bf16x2 b = __builtin_convertvector(f, bf16x2);
  return __builtin_bit_cast(unsigned, b);
}
__device__ __forceinline__ unsigned pk_int(unsigned lo, unsigned hi) { return pk_bf16((float)(int)lo, (float)(int)hi); }
__device__ __forceinline__ float bf_lo(unsigned u) { return __builtin_bit_cast(float, u << 16); }
__device__ __forceinline__ float bf_hi(unsigned u) { return __builtin_bit_cast(float, u & 0xffff0000u); }
template <int CTRL> __device__ __forceinline__ float dpp(float x) {
  return __builtin_bit_cast(float, __builtin_amdgcn_mov_dpp(__builtin_bit_cast(int, x), CTRL, 0xf, 0xf, true));
}
constexpr int XOR1 = 0xB1, XOR2 = 0x4E, HMIRROR = 0x141, MIRROR = 0x140;
__device__ __forceinline__ float x16_max(float x) {
  auto s = __builtin_amdgcn_permlane16_swap(__float_as_uint(x), __float_as_uint(x), false, false);
  return fmaxf(__uint_as_float(s[0]), __uint_as_float(s[1]));
}
__device__ __forceinline__ float x32_max(float x) {
  auto t = __builtin_amdgcn_permlane32_swap(__float_as_uint(x), __float_as_uint(x), false, false);
  return fmaxf(__uint_as_float(t[0]), __uint_as_float(t[1]));
}
__device__ __forceinline__ float x16_sum(float x) {
  auto s = __builtin_amdgcn_permlane16_swap(__float_as_uint(x), __float_as_uint(x), false, false);
  return __uint_as_float(s[0]) + __uint_as_float(s[1]);
}
__device__ __forceinline__ float x32_sum(float x) {
  auto t = __builtin_amdgcn_permlane32_swap(__float_as_uint(x), __float_as_uint(x), false, false);
  return __uint_as_float(t[0]) + __uint_as_float(t[1]);
}
__device__ __forceinline__ unsigned rfl(unsigned x) { return (unsigned)__builtin_amdgcn_readfirstlane((int)x); }
__device__ __forceinline__ f32x4 mfma16(u32x4 a, u32x4 b, f32x4 c) {
  return __builtin_amdgcn_mfma_f32_16x16x32_bf16(__builtin_bit_cast(bf16x8, a), __builtin_bit_cast(bf16x8, b), c, 0, 0, 0);
}
__device__ __forceinline__ u32x4 make_srd(const void* p, unsigned bytes) {
  return u32x4{(unsigned)(size_t)p, (unsigned)((size_t)p >> 32) & 0xffffu, bytes, 0x00020000u};
}
template <int OFF> __device__ __forceinline__ void ld_nt(u32x4& dst, unsigned voff, u32x4 srd, unsigned soff) {
  asm volatile("buffer_load_dwordx4 %0, %1, %2, %3 offen offset:%4 nt" : "=v"(dst) : "v"(voff), "s"(srd), "s"(soff), "i"(OFF));
}
__device__ __forceinline__ void ld_x4(u32x4& dst, unsigned voff, u32x4 srd, unsigned soff) {
  asm volatile("buffer_load_dwordx4 %0, %1, %2, %3 offen" : "=v"(dst) : "v"(voff), "s"(srd), "s"(soff));
}
template <int OFF> __device__ __forceinline__ void ld_x2(u32x2& dst, unsigned voff, u32x4 srd, unsigned soff) {
  asm volatile("buffer_load_dwordx2 %0, %1, %2, %3 offen offset:%4" : "=v"(dst) : "v"(voff), "s"(srd), "s"(soff), "i"(OFF));
}
#define WAIT2(N, a, b) asm volatile("s_waitcnt vmcnt(" #N ")" : "+v"(a), "+v"(b))
#define WAIT4(N, a, b, c, d) asm volatile("s_waitcnt vmcnt(" #N ")" : "+v"(a), "+v"(b), "+v"(c), "+v"(d))
#define PIN(x) asm volatile("" : "+v"(x))

constexpr int REC = 16 + GQ * D;

__global__ void __launch_bounds__(NTHR, WAVES / 4) attn_decode(const unsigned short* __restrict__ q, const int* __restrict__ qk,
                                                               const int* __restrict__ qv, const unsigned short* __restrict__ ks,
                                                               const unsigned short* __restrict__ kz, const unsigned short* __restrict__ vs,
                                                               const unsigned short* __restrict__ vz, unsigned char* __restrict__ ws,
                                                               unsigned short* __restrict__ out) {
  unsigned long long d0; asm volatile("s_memrealtime %0" : "=s"(d0)); __builtin_amdgcn_sched_barrier(0);
  __shared__ __attribute__((aligned(16))) float lds[WAVES * REC + 4];
  const int tid = threadIdx.x, lane = tid & 63;
  const int wave = __builtin_amdgcn_readfirstlane(tid >> 6);
  const int bid = blockIdx.x;
  const int hk = bid / NSPLIT, sp = bid % NSPLIT;
  const int grp = sp * TOK_WG / GTOK;
  const int g = lane >> 4, hq = (lane >> 2) & 3, cq = lane & 3;
  const bool c1 = (lane & 1) != 0, c2 = (lane & 2) != 0;

  asm volatile("s_waitcnt lgkmcnt(0)" :: "s"(q), "s"(qk), "s"(ws), "s"(d0));
  const unsigned long long dA = __builtin_amdgcn_s_memrealtime();
  asm volatile("s_waitcnt lgkmcnt(0)" :: "s"(dA));
  u32x2 qr0, qr1, ks0, ks1, kz0, kz1;
  u32x4 ctl;
  {
    const unsigned dq2 = (16 * g + 4 * cq) * 2;
    const u32x4 sq = make_srd(q, HQ * D * 2), sks = make_srd(ks, HKV * NGK * D * 2), skz = make_srd(kz, HKV * NGK * D * 2);
    const unsigned zero = 0;
    asm volatile("buffer_load_dwordx4 %0, %1, %2, %3 offen sc1" : "=v"(ctl) : "v"(zero), "s"(make_srd(ws, CTL_BYTES)), "s"(hk * 256));
    ld_x2<0>(qr0, dq2 + hq * (D * 2), sq, hk * (GQ * D * 2));   ld_x2<128>(qr1, dq2 + hq * (D * 2), sq, hk * (GQ * D * 2));
    ld_x2<0>(ks0, dq2, sks, (hk * NGK + grp) * (D * 2));        ld_x2<128>(ks1, dq2, sks, (hk * NGK + grp) * (D * 2));
    ld_x2<0>(kz0, dq2, skz, (hk * NGK + grp) * (D * 2));        ld_x2<128>(kz1, dq2, skz, (hk * NGK + grp) * (D * 2));
  }
  const unsigned long long dB0 = __builtin_amdgcn_s_memrealtime();
  asm volatile("s_waitcnt lgkmcnt(0)" :: "s"(dB0));
  const unsigned long long dB = __builtin_amdgcn_s_memrealtime();
  asm volatile("s_waitcnt lgkmcnt(0)" :: "s"(dB));
  u32x4 sk = make_srd(qk, HKV * SKV * D * 4u), sv = make_srd(qv, HKV * SKV * D * 4u);
  u32x4 svs = make_srd(vs, HKV * SKV * 2u), svz = make_srd(vz, HKV * SKV * 2u);
  const unsigned kofs = hq * (8 * D * 4) + g * 64 + cq * 16, vofs = g * (8 * D * 4) + (lane & 15) * 16, sofs = g * 16;
  unsigned row = hk * SKV + sp * TOK_WG + wave * (MAPV ? TILE : TILE * NTILE);
  u32x4 K[16], V[16], S, Z;
#define KLD(j) do { ld_nt<(j) * 512>(K[2 * (j)], kofs, sk, row * (D * 4)); ld_nt<(j) * 512 + 256>(K[2 * (j) + 1], kofs, sk, row * (D * 4)); } while (0)
#define VLD(j) do { ld_nt<(j) * 512>(V[2 * (j)], vofs, sv, row * (D * 4)); ld_nt<(j) * 512 + 256>(V[2 * (j) + 1], vofs, sv, row * (D * 4)); } while (0)
  KLD(0); KLD(1); KLD(2); KLD(3); KLD(4); KLD(5); KLD(6); KLD(7);
  ld_x4(S, sofs, svs, row * 2); ld_x4(Z, sofs, svz, row * 2);
  VLD(0); VLD(1); VLD(2); VLD(3); VLD(4); VLD(5); VLD(6); VLD(7);

  asm volatile("s_waitcnt vmcnt(34)" : "+v"(ctl), "+v"(qr0), "+v"(qr1), "+v"(ks0), "+v"(ks1), "+v"(kz0), "+v"(kz1));
  const unsigned long long epoch = ((unsigned long long)rfl(ctl[1]) << 32) | rfl(ctl[0]);
  const unsigned long long arr0  = ((unsigned long long)rfl(ctl[3]) << 32) | rfl(ctl[2]);
  const unsigned long long tag = (epoch * 0x5DEECE66Dull + 0xBull) & MASK48;
  gu64* const p_epoch = (gu64*)(ws + hk * 256);
  gu64* const p_arr = p_epoch + 1;
  if (tid == 0 && (arr0 >> 16) != tag) {
    unsigned long long expect = arr0;
    __hip_atomic_compare_exchange_strong(p_arr, &expect, tag << 16, __ATOMIC_RELAXED, __ATOMIC_RELAXED, __HIP_MEMORY_SCOPE_AGENT);
  }
  constexpr float C2 = 0.088388347648318440f * 1.4426950408889634f;
  u32x4 Qh, Ql; float bias = 0.f;
  {
    const unsigned qq[4] = {qr0[0], qr0[1], qr1[0], qr1[1]}, kk[4] = {ks0[0], ks0[1], ks1[0], ks1[1]}, zz[4] = {kz0[0], kz0[1], kz1[0], kz1[1]};
#pragma unroll
    for (int w = 0; w < 4; ++w) {
      const float a0 = bf_lo(qq[w]) * bf_lo(kk[w]) * C2, a1 = bf_hi(qq[w]) * bf_hi(kk[w]) * C2;
      const unsigned hi = pk_bf16(a0, a1);
      const unsigned lo = pk_bf16(a0 - bf_lo(hi), a1 - bf_hi(hi));
      Qh[w] = hi; Ql[w] = lo;
      bias = fmaf(-(bf_lo(hi) + bf_lo(lo)), bf_lo(zz[w]), bias);
      bias = fmaf(-(bf_hi(hi) + bf_hi(lo)), bf_hi(zz[w]), bias);
    }
  }
  bias += dpp<XOR1>(bias); bias += dpp<XOR2>(bias); bias = x32_sum(x16_sum(bias));

  asm volatile("s_waitcnt vmcnt(32)" :: "v"(bias));
  const unsigned long long dC = __builtin_amdgcn_s_memrealtime();
  float m = -INFINITY, lsum = 0.f, zsum = 0.f;
  f32x4 acc[8];
#pragma unroll
  for (int i = 0; i < 8; ++i) acc[i] = f32x4{0.f, 0.f, 0.f, 0.f};

#pragma unroll 1
  for (int it = 0; it < NTILE; ++it) {
    if (it + 1 < NTILE) row += MAPV ? WAVES * TILE : TILE;
    else { sk[2] = 0; sv[2] = 0; svs[2] = 0; svz[2] = 0; }
    float s[8];
#define QK(j) do { \
      WAIT2(32, K[2 * (j)], K[2 * (j) + 1]); \
      u32x4 a = {pk_int(K[2 * (j)][0], K[2 * (j)][1]), pk_int(K[2 * (j)][2], K[2 * (j)][3]), \
                 pk_int(K[2 * (j) + 1][0], K[2 * (j) + 1][1]), pk_int(K[2 * (j) + 1][2], K[2 * (j) + 1][3])}; \
      PIN(a); \
      KLD(j); \
      f32x4 d = mfma16(a, Qh, f32x4{0.f, 0.f, 0.f, 0.f}); \
      d = mfma16(a, Ql, d); \
      const float x0 = c1 ? d[1] : d[0], x1 = c1 ? d[3] : d[2]; \
      float x = c2 ? x1 : x0; \
      x += dpp<XOR1>(x); x += dpp<XOR2>(x); \
      s[j] = x; } while (0)
    QK(0); QK(1); QK(2); QK(3); QK(4); QK(5); QK(6); QK(7);
    float tm = fmaxf(fmaxf(fmaxf(s[0], s[1]), fmaxf(s[2], s[3])), fmaxf(fmaxf(s[4], s[5]), fmaxf(s[6], s[7])));
    tm = x32_max(x16_max(tm));
    const float mn = fmaxf(m, tm);
    const float alpha = __builtin_amdgcn_exp2f(m - mn);
    m = mn;
    WAIT2(32, S, Z);
    float w[8];
    float ls = 0.f, zs = 0.f;
#pragma unroll
    for (int j = 0; j < 8; ++j) {
      const float p = __builtin_amdgcn_exp2f(s[j] - mn);
      const float sc = (j & 1) ? bf_hi(S[j >> 1]) : bf_lo(S[j >> 1]);
      const float zz = (j & 1) ? bf_hi(Z[j >> 1]) : bf_lo(Z[j >> 1]);
      w[j] = p * sc; ls += p; zs = fmaf(w[j], zz, zs);
    }
    lsum = fmaf(lsum, alpha, ls); zsum = fmaf(zsum, alpha, zs);
    u32x4 P;
#pragma unroll
    for (int j = 0; j < 4; ++j) {
      const unsigned hi = pk_bf16(w[2 * j], w[2 * j + 1]);
      const unsigned lo = pk_bf16(w[2 * j] - bf_lo(hi), w[2 * j + 1] - bf_hi(hi));
      P[j] = c1 ? lo : hi;
    }
    asm volatile("" : "+v"(P), "+v"(zsum));
    ld_x4(S, sofs, svs, row * 2); ld_x4(Z, sofs, svz, row * 2);
    {
      int ai = __builtin_bit_cast(int, alpha), ag = ai;
      ag = __builtin_amdgcn_update_dpp(ag, ai, 0x150, 0x1, 0xf, false);
      ag = __builtin_amdgcn_update_dpp(ag, ai, 0x154, 0x2, 0xf, false);
      ag = __builtin_amdgcn_update_dpp(ag, ai, 0x158, 0x4, 0xf, false);
      ag = __builtin_amdgcn_update_dpp(ag, ai, 0x15C, 0x8, 0xf, false);
      const float af = __builtin_bit_cast(float, ag);
#pragma unroll
      for (int i = 0; i < 8; ++i) { acc[i][0] *= af; acc[i][1] *= af; }
    }
    u32x4 B[8];
#define PVC(jp) do { \
      WAIT4(30, V[4 * (jp)], V[4 * (jp) + 1], V[4 * (jp) + 2], V[4 * (jp) + 3]); \
      unsigned b0 = pk_int(V[4 * (jp)][0], V[4 * (jp) + 2][0]), b1 = pk_int(V[4 * (jp)][1], V[4 * (jp) + 2][1]), \
               b2 = pk_int(V[4 * (jp)][2], V[4 * (jp) + 2][2]), b3 = pk_int(V[4 * (jp)][3], V[4 * (jp) + 2][3]), \
               b4 = pk_int(V[4 * (jp) + 1][0], V[4 * (jp) + 3][0]), b5 = pk_int(V[4 * (jp) + 1][1], V[4 * (jp) + 3][1]), \
               b6 = pk_int(V[4 * (jp) + 1][2], V[4 * (jp) + 3][2]), b7 = pk_int(V[4 * (jp) + 1][3], V[4 * (jp) + 3][3]); \
      asm volatile("" : "+v"(b0), "+v"(b1), "+v"(b2), "+v"(b3), "+v"(b4), "+v"(b5), "+v"(b6), "+v"(b7)); \
      VLD(2 * (jp)); VLD(2 * (jp) + 1); \
      B[0][jp] = b0; B[1][jp] = b1; B[2][jp] = b2; B[3][jp] = b3; B[4][jp] = b4; B[5][jp] = b5; B[6][jp] = b6; B[7][jp] = b7; } while (0)
    PVC(0); PVC(1); PVC(2); PVC(3);
#pragma unroll
    for (int i = 0; i < 8; ++i) acc[i] = mfma16(P, B[i], acc[i]);
  }
  asm volatile("s_waitcnt vmcnt(0)" : "+v"(K[0]), "+v"(K[1]), "+v"(K[2]), "+v"(K[3]), "+v"(K[4]), "+v"(K[5]), "+v"(K[6]), "+v"(K[7]),
               "+v"(K[8]), "+v"(K[9]), "+v"(K[10]), "+v"(K[11]), "+v"(K[12]), "+v"(K[13]), "+v"(K[14]), "+v"(K[15]), "+v"(S), "+v"(Z));
  asm volatile("" : "+v"(V[0]), "+v"(V[1]), "+v"(V[2]), "+v"(V[3]), "+v"(V[4]), "+v"(V[5]), "+v"(V[6]), "+v"(V[7]),
               "+v"(V[8]), "+v"(V[9]), "+v"(V[10]), "+v"(V[11]), "+v"(V[12]), "+v"(V[13]), "+v"(V[14]), "+v"(V[15]));

  lsum = x32_sum(x16_sum(lsum)); zsum = x32_sum(x16_sum(zsum));
  float* rec = lds + wave * REC;
  if (lane < 16 && cq == 0) { rec[hq] = m + bias; rec[4 + hq] = lsum; rec[8 + hq] = zsum; }
#pragma unroll
  for (int h = 0; h < 2; ++h) {
    f32x4 o;
#pragma unroll
    for (int e = 0; e < 4; ++e) o[e] = acc[4 * h + e][0] + acc[4 * h + e][1];
    *reinterpret_cast<f32x4*>(rec + 16 + g * D + 64 * h + 4 * (lane & 15)) = o;
  }
  __syncthreads();
  const __amdgpu_buffer_rsrc_t rp = __builtin_amdgcn_make_buffer_rsrc(ws + CTL_BYTES, 0, HKV * NSPLIT * PREC * 4, 0x00020000);
  if (wave < 2) {
    const int h = tid >> 5;
    float M = -INFINITY;
#pragma unroll
    for (int wv = 0; wv < WAVES; ++wv) M = fmaxf(M, lds[wv * REC + h]);
    f32x4 O = {0.f, 0.f, 0.f, 0.f};
#pragma unroll
    for (int wv = 0; wv < WAVES; ++wv) {
      const float f = __builtin_amdgcn_exp2f(lds[wv * REC + h] - M), fz = -f * lds[wv * REC + 8 + h];
      const f32x4 o = *reinterpret_cast<const f32x4*>(lds + wv * REC + 16 + 4 * tid);
#pragma unroll
      for (int e = 0; e < 4; ++e) O[e] += fmaf(f, o[e], fz);
    }
    __builtin_amdgcn_raw_buffer_store_b128(__builtin_bit_cast(u32x4, O), rp, tid * 16, bid * (PREC * 4), 16);
  } else if (wave == 2) {
    const int h = lane & 3;
    float M = -INFINITY;
#pragma unroll
    for (int wv = 0; wv < WAVES; ++wv) M = fmaxf(M, lds[wv * REC + h]);
    float L = 0.f;
#pragma unroll
    for (int wv = 0; wv < WAVES; ++wv) L = fmaf(__builtin_amdgcn_exp2f(lds[wv * REC + h] - M), lds[wv * REC + 4 + h], L);
    if (lane < 8) __builtin_amdgcn_raw_buffer_store_b32(__builtin_bit_cast(unsigned, lane < 4 ? M : L), rp, (GQ * D + lane) * 4, bid * (PREC * 4), 16);
  }
  asm volatile("s_waitcnt vmcnt(0)" ::: "memory");
  __syncthreads();
  if (tid == 0) {
    const unsigned long long old = __hip_atomic_fetch_add(p_arr, 1ull, __ATOMIC_RELAXED, __HIP_MEMORY_SCOPE_AGENT);
    const bool last = (old & 0xffffu) == NSPLIT - 1;
    if (last) __hip_atomic_store(p_epoch, tag, __ATOMIC_RELAXED, __HIP_MEMORY_SCOPE_AGENT);
    lds[WAVES * REC] = last ? 1.f : 0.f;
  }
  __syncthreads();
  if (lds[WAVES * REC] != 0.f && wave < GQ) {
    const int h = wave, half = lane >> 5;
    const unsigned rb = hk * NSPLIT * (PREC * 4);
    const float ml = __builtin_bit_cast(float, __builtin_amdgcn_raw_buffer_load_b32(rp, ((lane & 31) * PREC + GQ * D + 4 * half + h) * 4, rb, 16));
    f32x4 o[16]; float mr[16];
#pragma unroll
    for (int r = 0; r < 16; ++r) {
      mr[r] = __builtin_bit_cast(float, __builtin_amdgcn_raw_buffer_load_b32(rp, ((half * 16 + r) * PREC + GQ * D + h) * 4, rb, 16));
      o[r] = __builtin_bit_cast(f32x4, __builtin_amdgcn_raw_buffer_load_b128(rp, ((half * 16 + r) * PREC + h * D + 4 * (lane & 31)) * 4, rb, 16));
    }
    auto sw = __builtin_amdgcn_permlane32_swap(__float_as_uint(ml), __float_as_uint(ml), false, false);
    const float mi = __uint_as_float(sw[0]), li = __uint_as_float(sw[1]);
    float M = mi;
    M = fmaxf(M, dpp<XOR1>(M)); M = fmaxf(M, dpp<XOR2>(M)); M = fmaxf(M, dpp<HMIRROR>(M)); M = fmaxf(M, dpp<MIRROR>(M)); M = x16_max(M);
    float L = __builtin_amdgcn_exp2f(mi - M) * li;
    L += dpp<XOR1>(L); L += dpp<XOR2>(L); L += dpp<HMIRROR>(L); L += dpp<MIRROR>(L); L = x16_sum(L);
    f32x4 O = {0.f, 0.f, 0.f, 0.f};
#pragma unroll
    for (int r = 0; r < 16; ++r) {
      const float f = __builtin_amdgcn_exp2f(mr[r] - M);
#pragma unroll
      for (int e = 0; e < 4; ++e) O[e] = fmaf(f, o[r][e], O[e]);
    }
    const float inv = 1.f / L;
#pragma unroll
    for (int e = 0; e < 4; ++e) O[e] = x32_sum(O[e]) * inv;
    if (lane < 32) *reinterpret_cast<u32x2*>(out + (hk * GQ + h) * D + 4 * lane) = u32x2{pk_bf16(O[0], O[1]), pk_bf16(O[2], O[3])};
    if (wave == 0) { asm volatile("s_waitcnt vmcnt(0)" ::: "memory"); const unsigned long long d3 = __builtin_amdgcn_s_memrealtime();
      const unsigned long long until = d3 + (0ull);
      while (__builtin_amdgcn_s_memrealtime() < until) __builtin_amdgcn_s_sleep(1); }
  }
}

}

extern "C" void kernel_launch(void* const* d_in, const int* in_sizes, int n_in, void* d_out, int out_size, void* d_ws,
                              size_t ws_size, hipStream_t stream) {
  attn_decode<<<HKV * NSPLIT, NTHR, 0, stream>>>((const unsigned short*)d_in[0], (const int*)d_in[1], (const int*)d_in[2],
                                                 (const unsigned short*)d_in[3], (const unsigned short*)d_in[4],
                                                 (const unsigned short*)d_in[5], (const unsigned short*)d_in[6],
                                                 (unsigned char*)d_ws, (unsigned short*)d_out);
}
```

```cpp
#include <hip/hip_runtime.h>
#include <hip/hip_bf16.h>
#include <stdint.h>
#include <type_traits>

#define WAVES 8
#define MAPV 1
#define STAMP_OFF (64u << 20)
#define STAMP_N (HKV * NSPLIT * WAVES)

namespace {

constexpr int HQ = 32, HKV = 8, GQ = HQ / HKV, SKV = 32768, D = 128, NGK = 2, GTOK = SKV / NGK;
constexpr int NSPLIT = 32;
constexpr int NTHR = WAVES * 64;
constexpr int TOK_WG = SKV / NSPLIT;
constexpr int TILE = 32;
constexpr int NTILE = TOK_WG / (WAVES * TILE);
static_assert((WAVES == 4 || WAVES == 8) && GQ == 4 && TOK_WG % (WAVES * TILE) == 0 && GTOK % TOK_WG == 0 && WAVES >= 4, "shape");

constexpr int CTL_BYTES = 4096;
constexpr int RECB = 1024 + 128;
constexpr int RPW = NSPLIT / WAVES;

typedef unsigned u32x4 __attribute__((ext_vector_type(4)));
typedef unsigned u32x2 __attribute__((ext_vector_type(2)));
typedef float  f32x4 __attribute__((ext_vector_type(4)));
typedef float  f32x2 __attribute__((ext_vector_type(2)));
typedef __bf16 bf16x8 __attribute__((ext_vector_type(8)));
typedef __bf16 bf16x2 __attribute__((ext_vector_type(2)));
typedef __attribute__((address_space(1))) unsigned gu32;

__device__ __forceinline__ unsigned pk_bf16(float lo, float hi) {
  f32x2 f = {lo, hi};
  bf16x2 b = __builtin_convertvector(f, bf16x2);
  return __builtin_bit_cast(unsigned, b);
}
__device__ __forceinline__ unsigned pk_int(unsigned lo, unsigned hi) { return pk_bf16((float)(int)lo, (float)(int)hi); }
__device__ __forceinline__ float bf_lo(unsigned u) { return __builtin_bit_cast(float, u << 16); }
__device__ __forceinline__ float bf_hi(unsigned u) { return __builtin_bit_cast(float, u & 0xffff0000u); }
template <int CTRL> __device__ __forceinline__ float dpp(float x) {
  return __builtin_bit_cast(float, __builtin_amdgcn_mov_dpp(__builtin_bit_cast(int, x), CTRL, 0xf, 0xf, true));
}
constexpr int XOR1 = 0xB1, XOR2 = 0x4E, HMIRROR = 0x141, MIRROR = 0x140;
__device__ __forceinline__ float x16_max(float x) {
  auto s = __builtin_amdgcn_permlane16_swap(__float_as_uint(x), __float_as_uint(x), false, false);
  return fmaxf(__uint_as_float(s[0]), __uint_as_float(s[1]));
}
__device__ __forceinline__ float x32_max(float x) {
  auto t = __builtin_amdgcn_permlane32_swap(__float_as_uint(x), __float_as_uint(x), false, false);
  return fmaxf(__uint_as_float(t[0]), __uint_as_float(t[1]));
}
__device__ __forceinline__ float x16_sum(float x) {
  auto s = __builtin_amdgcn_permlane16_swap(__float_as_uint(x), __float_as_uint(x), false, false);
  return __uint_as_float(s[0]) + __uint_as_float(s[1]);
}
__device__ __forceinline__ float x32_sum(float x) {
  auto t = __builtin_amdgcn_permlane32_swap(__float_as_uint(x), __float_as_uint(x), false, false);
  return __uint_as_float(t[0]) + __uint_as_float(t[1]);
}
__device__ __forceinline__ unsigned rfl(unsigned x) { return (unsigned)__builtin_amdgcn_readfirstlane((int)x); }
__device__ __forceinline__ f32x4 mfma16(u32x4 a, u32x4 b, f32x4 c) {
  return __builtin_amdgcn_mfma_f32_16x16x32_bf16(__builtin_bit_cast(bf16x8, a), __builtin_bit_cast(bf16x8, b), c, 0, 0, 0);
}
__device__ __forceinline__ u32x4 make_srd(const void* p, unsigned bytes) {
  return u32x4{(unsigned)(size_t)p, (unsigned)((size_t)p >> 32) & 0xffffu, bytes, 0x00020000u};
}
template <int OFF> __device__ __forceinline__ void ld_nt(u32x4& dst, unsigned voff, u32x4 srd, unsigned soff) {
  asm volatile("buffer_load_dwordx4 %0, %1, %2, %3 offen offset:%4 nt" : "=v"(dst) : "v"(voff), "s"(srd), "s"(soff), "i"(OFF));
}
template <int OFF> __device__ __forceinline__ void gld_nt(u32x4& dst, unsigned voff, const char* base) {
  asm volatile("global_load_dwordx4 %0, %1, %2 offset:%3 nt" : "=v"(dst) : "v"(voff), "s"(base), "i"(OFF));
}
__device__ __forceinline__ void ld_x4(u32x4& dst, unsigned voff, u32x4 srd, unsigned soff) {
  asm volatile("buffer_load_dwordx4 %0, %1, %2, %3 offen nt" : "=v"(dst) : "v"(voff), "s"(srd), "s"(soff));
}
template <int OFF> __device__ __forceinline__ void ld_x2(u32x2& dst, unsigned voff, u32x4 srd, unsigned soff) {
  asm volatile("buffer_load_dwordx2 %0, %1, %2, %3 offen offset:%4" : "=v"(dst) : "v"(voff), "s"(srd), "s"(soff), "i"(OFF));
}
__device__ __forceinline__ void st_rec(u32x4 v, unsigned voff, u32x4 srd, unsigned soff) {
  asm volatile("buffer_store_dwordx4 %0, %1, %2, %3 offen sc1\n\ts_nop 1" :: "v"(v), "v"(voff), "s"(srd), "s"(soff) : "memory");
}
__device__ __forceinline__ void ld_rec(u32x4& dst, unsigned voff, u32x4 srd, unsigned soff) {
  asm volatile("buffer_load_dwordx4 %0, %1, %2, %3 offen sc1" : "+v"(dst) : "v"(voff), "s"(srd), "s"(soff) : "memory");
}
#define WAIT2(N, a, b) asm volatile("s_waitcnt vmcnt(" #N ")" : "+v"(a), "+v"(b))
#define WAIT4(N, a, b, c, d) asm volatile("s_waitcnt vmcnt(" #N ")" : "+v"(a), "+v"(b), "+v"(c), "+v"(d))
#define WAITN2(N, a, b) asm volatile("s_waitcnt vmcnt(%2)" : "+v"(a), "+v"(b) : "i"(N))
#define WAITN4(N, a, b, c, d) asm volatile("s_waitcnt vmcnt(%4)" : "+v"(a), "+v"(b), "+v"(c), "+v"(d) : "i"(N))
#define PIN(x) asm volatile("" : "+v"(x))

constexpr int REC = 16 + GQ * D;

__global__ void __launch_bounds__(NTHR, 2) attn_decode(const unsigned short* __restrict__ q, const int* __restrict__ qk,
                                                               const int* __restrict__ qv, const unsigned short* __restrict__ ks,
                                                               const unsigned short* __restrict__ kz, const unsigned short* __restrict__ vs,
                                                               const unsigned short* __restrict__ vz, unsigned char* __restrict__ ws,
                                                               unsigned short* __restrict__ out) {
  unsigned long long d0; asm volatile("s_memrealtime %0" : "=s"(d0)); __builtin_amdgcn_sched_barrier(0);
  __shared__ __attribute__((aligned(16))) float lds[WAVES * REC + WAVES * 132];
  const int tid = threadIdx.x, lane = tid & 63;
  const int wave = __builtin_amdgcn_readfirstlane(tid >> 6);
  const int bid = blockIdx.x;
  const int hk = bid / NSPLIT, sp = bid % NSPLIT;
  const int grp = sp * TOK_WG / GTOK;
  const int g = lane >> 4, hq = (lane >> 2) & 3, cq = lane & 3;
  const bool c1 = (lane & 1) != 0, c2 = (lane & 2) != 0;

  u32x2 qr0, qr1, ks0, ks1, kz0, kz1;
  u32x4 ctl;
  {
    const unsigned dq2 = (16 * g + 4 * cq) * 2;
    const u32x4 sq = make_srd(q, HQ * D * 2), sks = make_srd(ks, HKV * NGK * D * 2), skz = make_srd(kz, HKV * NGK * D * 2);
    const unsigned zero = 0;
    asm volatile("buffer_load_dwordx4 %0, %1, %2, %3 offen sc1" : "=v"(ctl) : "v"(zero), "s"(make_srd(ws, CTL_BYTES)), "s"(hk * 128));
    ld_x2<0>(qr0, dq2 + hq * (D * 2), sq, hk * (GQ * D * 2));   ld_x2<128>(qr1, dq2 + hq * (D * 2), sq, hk * (GQ * D * 2));
    ld_x2<0>(ks0, dq2, sks, (hk * NGK + grp) * (D * 2));        ld_x2<128>(ks1, dq2, sks, (hk * NGK + grp) * (D * 2));
    ld_x2<0>(kz0, dq2, skz, (hk * NGK + grp) * (D * 2));        ld_x2<128>(kz1, dq2, skz, (hk * NGK + grp) * (D * 2));
  }
  u32x4 sk = make_srd(qk, HKV * SKV * D * 4u), sv = make_srd(qv, HKV * SKV * D * 4u);
  u32x4 svs = make_srd(vs, HKV * SKV * 2u), svz = make_srd(vz, HKV * SKV * 2u);
  const unsigned kofs = hq * (8 * D * 4) + g * 64 + cq * 16, vofs = g * (8 * D * 4) + (lane & 15) * 16, sofs = g * 16;
  unsigned row = hk * SKV + sp * TOK_WG + wave * (MAPV ? TILE : TILE * NTILE);
  u32x4 K[16], V[16], S, Z;
#define KLD(j) do { ld_nt<(j) * 512>(K[2 * (j)], kofs, sk, row * (D * 4)); ld_nt<(j) * 512 + 256>(K[2 * (j) + 1], kofs, sk, row * (D * 4)); } while (0)
#define VLD(j) do { ld_nt<(j) * 512>(V[2 * (j)], vofs, sv, row * (D * 4)); ld_nt<(j) * 512 + 256>(V[2 * (j) + 1], vofs, sv, row * (D * 4)); } while (0)
  KLD(0); KLD(1); KLD(2); KLD(3); KLD(4); KLD(5); KLD(6); KLD(7);
  ld_x4(S, sofs, svs, row * 2); ld_x4(Z, sofs, svz, row * 2);
  VLD(0); VLD(1); VLD(2); VLD(3); VLD(4); VLD(5); VLD(6); VLD(7);
  __builtin_amdgcn_sched_barrier(0);
  asm volatile("s_waitcnt vmcnt(34)" : "+v"(ctl), "+v"(qr0), "+v"(qr1), "+v"(ks0), "+v"(ks1), "+v"(kz0), "+v"(kz1));
  const u32x4 srec = make_srd(ws + CTL_BYTES, HQ * NSPLIT * RECB);
  const unsigned myrec = ((hk * GQ + (wave & 3)) * NSPLIT + sp) * RECB;
  if (wave < GQ) {
    const u32x4 z4 = {0u, 0u, 0u, 0u};
    st_rec(z4, lane * 16, srec, myrec);
    if (lane == 0) st_rec(z4, 1024, srec, myrec);
  }
  unsigned tagw = 0, tagf = 0;
#pragma unroll
  for (int i = 0; i < GQ; ++i) {
    const unsigned t = (rfl(ctl[i]) * 1664525u + 1013904221u) | 1u;
    if (wave == i) tagw = t;
    if (sp == i) tagf = t;
  }
  constexpr float C2 = 0.088388347648318440f * 1.4426950408889634f;
  u32x4 Qh, Ql; float bias = 0.f;
  {
    const unsigned qq[4] = {qr0[0], qr0[1], qr1[0], qr1[1]}, kk[4] = {ks0[0], ks0[1], ks1[0], ks1[1]}, zz[4] = {kz0[0], kz0[1], kz1[0], kz1[1]};
#pragma unroll
    for (int w = 0; w < 4; ++w) {
      const float a0 = bf_lo(qq[w]) * bf_lo(kk[w]) * C2, a1 = bf_hi(qq[w]) * bf_hi(kk[w]) * C2;
      const unsigned hi = pk_bf16(a0, a1);
      const unsigned lo = pk_bf16(a0 - bf_lo(hi), a1 - bf_hi(hi));
      Qh[w] = hi; Ql[w] = lo;
      bias = fmaf(-(bf_lo(hi) + bf_lo(lo)), bf_lo(zz[w]), bias);
      bias = fmaf(-(bf_hi(hi) + bf_hi(lo)), bf_hi(zz[w]), bias);
    }
  }
  bias += dpp<XOR1>(bias); bias += dpp<XOR2>(bias); bias = x32_sum(x16_sum(bias));

  float m = -INFINITY, lsum = 0.f, zsum = 0.f;
  f32x4 acc[8];
#pragma unroll
  for (int i = 0; i < 8; ++i) acc[i] = f32x4{0.f, 0.f, 0.f, 0.f};

  auto tile = [&](auto last_tag) __attribute__((always_inline)) {
    constexpr bool LAST = decltype(last_tag)::value;
    float s[8];
#define QK(j) do { \
      WAITN2(LAST ? 32 - 2 * (j) : 32, K[2 * (j)], K[2 * (j) + 1]); \
      u32x4 a = {pk_int(K[2 * (j)][0], K[2 * (j)][1]), pk_int(K[2 * (j)][2], K[2 * (j)][3]), \
                 pk_int(K[2 * (j) + 1][0], K[2 * (j) + 1][1]), pk_int(K[2 * (j) + 1][2], K[2 * (j) + 1][3])}; \
      PIN(a); \
      if (!LAST) KLD(j); \
      f32x4 d = mfma16(a, Qh, f32x4{0.f, 0.f, 0.f, 0.f}); \
      d = mfma16(a, Ql, d); \
      const float x0 = c1 ? d[1] : d[0], x1 = c1 ? d[3] : d[2]; \
      float x = c2 ? x1 : x0; \
      x += dpp<XOR1>(x); x += dpp<XOR2>(x); \
      s[j] = x; } while (0)
    QK(0); QK(1); QK(2); QK(3); QK(4); QK(5); QK(6); QK(7);
    float tm = fmaxf(fmaxf(fmaxf(s[0], s[1]), fmaxf(s[2], s[3])), fmaxf(fmaxf(s[4], s[5]), fmaxf(s[6], s[7])));
    tm = x32_max(x16_max(tm));
    const float mn = fmaxf(m, tm);
    const float alpha = __builtin_amdgcn_exp2f(m - mn);
    m = mn;
    WAITN2(LAST ? 16 : 32, S, Z);
    float w[8];
    float ls = 0.f, zs = 0.f;
#pragma unroll
    for (int j = 0; j < 8; ++j) {
      const float p = __builtin_amdgcn_exp2f(s[j] - mn);
      const float sc = (j & 1) ? bf_hi(S[j >> 1]) : bf_lo(S[j >> 1]);
      const float zz = (j & 1) ? bf_hi(Z[j >> 1]) : bf_lo(Z[j >> 1]);
      w[j] = p * sc; ls += p; zs = fmaf(w[j], zz, zs);
    }
    lsum = fmaf(lsum, alpha, ls); zsum = fmaf(zsum, alpha, zs);
    u32x4 P;
#pragma unroll
    for (int j = 0; j < 4; ++j) {
      const unsigned hi = pk_bf16(w[2 * j], w[2 * j + 1]);
      const unsigned lo = pk_bf16(w[2 * j] - bf_lo(hi), w[2 * j + 1] - bf_hi(hi));
      P[j] = c1 ? lo : hi;
    }
    asm volatile("" : "+v"(P), "+v"(zsum));
    if (!LAST) { ld_x4(S, sofs, svs, row * 2); ld_x4(Z, sofs, svz, row * 2); }
    {
      int ai = __builtin_bit_cast(int, alpha), ag = ai;
      ag = __builtin_amdgcn_update_dpp(ag, ai, 0x150, 0x1, 0xf, false);
      ag = __builtin_amdgcn_update_dpp(ag, ai, 0x154, 0x2, 0xf, false);
      ag = __builtin_amdgcn_update_dpp(ag, ai, 0x158, 0x4, 0xf, false);
      ag = __builtin_amdgcn_update_dpp(ag, ai, 0x15C, 0x8, 0xf, false);
      const float af = __builtin_bit_cast(float, ag);
#pragma unroll
      for (int i = 0; i < 8; ++i) { acc[i][0] *= af; acc[i][1] *= af; }
    }
    u32x4 B[8];
#define PVC(jp) do { \
      WAITN4(LAST ? 12 - 4 * (jp) : 30, V[4 * (jp)], V[4 * (jp) + 1], V[4 * (jp) + 2], V[4 * (jp) + 3]); \
      unsigned b0 = pk_int(V[4 * (jp)][0], V[4 * (jp) + 2][0]), b1 = pk_int(V[4 * (jp)][1], V[4 * (jp) + 2][1]), \
               b2 = pk_int(V[4 * (jp)][2], V[4 * (jp) + 2][2]), b3 = pk_int(V[4 * (jp)][3], V[4 * (jp) + 2][3]), \
               b4 = pk_int(V[4 * (jp) + 1][0], V[4 * (jp) + 3][0]), b5 = pk_int(V[4 * (jp) + 1][1], V[4 * (jp) + 3][1]), \
               b6 = pk_int(V[4 * (jp) + 1][2], V[4 * (jp) + 3][2]), b7 = pk_int(V[4 * (jp) + 1][3], V[4 * (jp) + 3][3]); \
      asm volatile("" : "+v"(b0), "+v"(b1), "+v"(b2), "+v"(b3), "+v"(b4), "+v"(b5), "+v"(b6), "+v"(b7)); \
      if (!LAST) { VLD(2 * (jp)); VLD(2 * (jp) + 1); } \
      B[0][jp] = b0; B[1][jp] = b1; B[2][jp] = b2; B[3][jp] = b3; B[4][jp] = b4; B[5][jp] = b5; B[6][jp] = b6; B[7][jp] = b7; } while (0)
    PVC(0); PVC(1); PVC(2); PVC(3);
#pragma unroll
    for (int i = 0; i < 8; ++i) acc[i] = mfma16(P, B[i], acc[i]);
  };
#pragma unroll 1
  for (int it = 0; it + 1 < NTILE; ++it) {
    row += MAPV ? WAVES * TILE : TILE;
    tile(std::false_type{});
  }
  tile(std::true_type{});

  asm volatile("" :: "v"(acc[0]), "v"(acc[7]));
  const unsigned long long d2 = __builtin_amdgcn_s_memrealtime();
  lsum = x32_sum(x16_sum(lsum)); zsum = x32_sum(x16_sum(zsum));
  float* rec = lds + wave * REC;
  if (lane < 16 && cq == 0) { rec[hq] = m + bias; rec[4 + hq] = lsum; rec[8 + hq] = zsum; }
#pragma unroll
  for (int h = 0; h < 2; ++h) {
    f32x4 o;
#pragma unroll
    for (int e = 0; e < 4; ++e) o[e] = acc[4 * h + e][0] + acc[4 * h + e][1];
    *reinterpret_cast<f32x4*>(rec + 16 + g * D + 64 * h + 4 * (lane & 15)) = o;
  }
  __syncthreads();
  if (wave < GQ) {
    const int h = wave;
    float M = -INFINITY;
#pragma unroll
    for (int wv = 0; wv < WAVES; ++wv) M = fmaxf(M, lds[wv * REC + h]);
    float L = 0.f, o0 = 0.f, o1 = 0.f;
#pragma unroll
    for (int wv = 0; wv < WAVES; ++wv) {
      const float f = __builtin_amdgcn_exp2f(lds[wv * REC + h] - M), fz = -f * lds[wv * REC + 8 + h];
      const f32x2 o = *reinterpret_cast<const f32x2*>(lds + wv * REC + 16 + h * D + 2 * lane);
      L = fmaf(f, lds[wv * REC + 4 + h], L);
      o0 += fmaf(f, o[0], fz); o1 += fmaf(f, o[1], fz);
    }
    st_rec(u32x4{__float_as_uint(o0), tagw, __float_as_uint(o1), tagw}, lane * 16, srec, myrec);
    if (lane == 0) st_rec(u32x4{__float_as_uint(M), tagw, __float_as_uint(L), tagw}, 1024, srec, myrec);
    if (lane == 0) st_rec(u32x4{(unsigned)d0, tagw, (unsigned)__builtin_amdgcn_s_memrealtime(), tagw}, 1040, srec, myrec);
  }
  if (sp < GQ) {
    const unsigned rb = ((hk * GQ + sp) * NSPLIT + wave * RPW) * RECB;
    u32x4 R[RPW], ML = {0u, 0u, 0u, 0u};
#pragma unroll
    for (int r = 0; r < RPW; ++r) R[r] = u32x4{0u, 0u, 0u, 0u};
    unsigned pend = (1u << RPW) - 1u;
    for (unsigned spins = 0; spins < (1u << 20); ++spins) {
      ld_rec(ML, (lane & (RPW - 1)) * RECB + 1024, srec, rb);
#pragma unroll
      for (int r = 0; r < RPW; ++r) if (pend & (1u << r)) ld_rec(R[r], lane * 16, srec, rb + r * RECB);
      asm volatile("s_waitcnt vmcnt(0)" : "+v"(ML));
#pragma unroll
      for (int r = 0; r < RPW; ++r) { PIN(R[r]); if (__all(R[r][1] == tagf && R[r][3] == tagf)) pend &= ~(1u << r); }
      if (pend == 0 && __all(ML[1] == tagf && ML[3] == tagf)) break;
      __builtin_amdgcn_s_sleep(2);
    }
    const float mi = __uint_as_float(ML[0]), li = __uint_as_float(ML[2]);
    float M = mi;
    M = fmaxf(M, dpp<XOR1>(M)); M = fmaxf(M, dpp<XOR2>(M));
    if (RPW == 8) M = fmaxf(M, dpp<HMIRROR>(M));
    const float fi = __builtin_amdgcn_exp2f(mi - M);
    float L = fi * li;
    L += dpp<XOR1>(L); L += dpp<XOR2>(L);
    if (RPW == 8) L += dpp<HMIRROR>(L);
    float o0 = 0.f, o1 = 0.f;
#pragma unroll
    for (int r = 0; r < RPW; ++r) {
      const float f = __builtin_bit_cast(float, __builtin_amdgcn_readlane(__builtin_bit_cast(int, fi), r));
      o0 = fmaf(f, __uint_as_float(R[r][0]), o0); o1 = fmaf(f, __uint_as_float(R[r][2]), o1);
    }
    float* fin = lds + WAVES * REC;
    if (lane == 0) { fin[wave * 132] = M; fin[wave * 132 + 1] = L; }
    *reinterpret_cast<f32x2*>(fin + wave * 132 + 4 + 2 * lane) = f32x2{o0, o1};
    __syncthreads();
    if (wave == 0) {
      float MM = -INFINITY;
#pragma unroll
      for (int wv = 0; wv < WAVES; ++wv) MM = fmaxf(MM, fin[wv * 132]);
      float LL = 0.f, a0 = 0.f, a1 = 0.f;
#pragma unroll
      for (int wv = 0; wv < WAVES; ++wv) {
        const float f = __builtin_amdgcn_exp2f(fin[wv * 132] - MM);
        const f32x2 o = *reinterpret_cast<const f32x2*>(fin + wv * 132 + 4 + 2 * lane);
        LL = fmaf(f, fin[wv * 132 + 1], LL); a0 = fmaf(f, o[0], a0); a1 = fmaf(f, o[1], a1);
      }
      const float inv = 1.f / LL;
      *reinterpret_cast<unsigned*>(out + (hk * GQ + sp) * D + 2 * lane) = pk_bf16(a0 * inv, a1 * inv);
      { u32x4 T = {0u, 0u, 0u, 0u};
        for (int k = 0; k < 100000; ++k) { ld_rec(T, (lane & 31) * RECB + 1040, srec, ((hk * GQ + sp) * NSPLIT) * RECB); asm volatile("s_waitcnt vmcnt(0)" : "+v"(T)); if (__all(T[1] == tagf && T[3] == tagf)) break; }
        int rel = (int)(T[0] - (unsigned)d0), relp = (int)(T[2] - (unsigned)d0);
        int mn = rel, mx = rel, pmn = relp, pmx = relp;
        for (int o = 1; o < 32; o <<= 1) { mn = min(mn, __shfl_xor(mn, o)); mx = max(mx, __shfl_xor(mx, o)); pmn = min(pmn, __shfl_xor(pmn, o)); pmx = max(pmx, __shfl_xor(pmx, o)); }
        mn = __builtin_amdgcn_readfirstlane(mn); mx = __builtin_amdgcn_readfirstlane(mx); pmn = __builtin_amdgcn_readfirstlane(pmn); pmx = __builtin_amdgcn_readfirstlane(pmx);
        const unsigned long long d3 = __builtin_amdgcn_s_memrealtime();
        const unsigned long long until = d3 + (unsigned long long)((long long)(d3 - d0) - mn);
        while (__builtin_amdgcn_s_memrealtime() < until) __builtin_amdgcn_s_sleep(1); }
      if (lane == 0) __hip_atomic_store((gu32*)(ws + hk * 128 + sp * 4), tagf, __ATOMIC_RELAXED, __HIP_MEMORY_SCOPE_AGENT);
    }
  }
}

}

extern "C" void kernel_launch(void* const* d_in, const int* in_sizes, int n_in, void* d_out, int out_size, void* d_ws,
                              size_t ws_size, hipStream_t stream) {
  attn_decode<<<HKV * NSPLIT, NTHR, 0, stream>>>((const unsigned short*)d_in[0], (const int*)d_in[1], (const int*)d_in[2],
                                                 (const unsigned short*)d_in[3], (const unsigned short*)d_in[4],
                                                 (const unsigned short*)d_in[5], (const unsigned short*)d_in[6],
                                                 (unsigned char*)d_ws, (unsigned short*)d_out);
}
```

```cpp
#include <hip/hip_runtime.h>
#include <hip/hip_bf16.h>
#include <stdint.h>
#include <type_traits>

#define WAVES 8
#define MAPV 1
#define STAMP_OFF (64u << 20)
#define STAMP_N (HKV * NSPLIT * WAVES)

namespace {

constexpr int HQ = 32, HKV = 8, GQ = HQ / HKV, SKV = 32768, D = 128, NGK = 2, GTOK = SKV / NGK;
constexpr int NSPLIT = 32;
constexpr int NTHR = WAVES * 64;
constexpr int TOK_WG = SKV / NSPLIT;
constexpr int TILE = 32;
constexpr int NTILE = TOK_WG / (WAVES * TILE);
static_assert((WAVES == 4 || WAVES == 8) && GQ == 4 && TOK_WG % (WAVES * TILE) == 0 && GTOK % TOK_WG == 0 && WAVES >= 4, "shape");

constexpr int CTL_BYTES = 4096;
constexpr int RECB = 1024 + 128;
constexpr int RPW = NSPLIT / WAVES;

typedef unsigned u32x4 __attribute__((ext_vector_type(4)));
typedef unsigned u32x2 __attribute__((ext_vector_type(2)));
typedef float  f32x4 __attribute__((ext_vector_type(4)));
typedef float  f32x2 __attribute__((ext_vector_type(2)));
typedef __bf16 bf16x8 __attribute__((ext_vector_type(8)));
typedef __bf16 bf16x2 __attribute__((ext_vector_type(2)));
typedef __attribute__((address_space(1))) unsigned gu32;

__device__ __forceinline__ unsigned pk_bf16(float lo, float hi) {
  f32x2 f = {lo, hi};
  bf16x2 b = __builtin_convertvector(f, bf16x2);
  return __builtin_bit_cast(unsigned, b);
}
__device__ __forceinline__ unsigned pk_int(unsigned lo, unsigned hi) { return pk_bf16((float)(int)lo, (float)(int)hi); }
__device__ __forceinline__ float bf_lo(unsigned u) { return __builtin_bit_cast(float, u << 16); }
__device__ __forceinline__ float bf_hi(unsigned u) { return __builtin_bit_cast(float, u & 0xffff0000u); }
template <int CTRL> __device__ __forceinline__ float dpp(float x) {
  return __builtin_bit_cast(float, __builtin_amdgcn_mov_dpp(__builtin_bit_cast(int, x), CTRL, 0xf, 0xf, true));
}
constexpr int XOR1 = 0xB1, XOR2 = 0x4E, HMIRROR = 0x141, MIRROR = 0x140;
__device__ __forceinline__ float x16_max(float x) {
  auto s = __builtin_amdgcn_permlane16_swap(__float_as_uint(x), __float_as_uint(x), false, false);
  return fmaxf(__uint_as_float(s[0]), __uint_as_float(s[1]));
}
__device__ __forceinline__ float x32_max(float x) {
  auto t = __builtin_amdgcn_permlane32_swap(__float_as_uint(x), __float_as_uint(x), false, false);
  return fmaxf(__uint_as_float(t[0]), __uint_as_float(t[1]));
}
__device__ __forceinline__ float x16_sum(float x) {
  auto s = __builtin_amdgcn_permlane16_swap(__float_as_uint(x), __float_as_uint(x), false, false);
  return __uint_as_float(s[0]) + __uint_as_float(s[1]);
}
__device__ __forceinline__ float x32_sum(float x) {
  auto t = __builtin_amdgcn_permlane32_swap(__float_as_uint(x), __float_as_uint(x), false, false);
  return __uint_as_float(t[0]) + __uint_as_float(t[1]);
}
__device__ __forceinline__ unsigned rfl(unsigned x) { return (unsigned)__builtin_amdgcn_readfirstlane((int)x); }
__device__ __forceinline__ f32x4 mfma16(u32x4 a, u32x4 b, f32x4 c) {
  return __builtin_amdgcn_mfma_f32_16x16x32_bf16(__builtin_bit_cast(bf16x8, a), __builtin_bit_cast(bf16x8, b), c, 0, 0, 0);
}
__device__ __forceinline__ u32x4 make_srd(const void* p, unsigned bytes) {
  return u32x4{(unsigned)(size_t)p, (unsigned)((size_t)p >> 32) & 0xffffu, bytes, 0x00020000u};
}
template <int OFF> __device__ __forceinline__ void ld_nt(u32x4& dst, unsigned voff, u32x4 srd, unsigned soff) {
  asm volatile("buffer_load_dwordx4 %0, %1, %2, %3 offen offset:%4 nt" : "=v"(dst) : "v"(voff), "s"(srd), "s"(soff), "i"(OFF));
}
template <int OFF> __device__ __forceinline__ void gld_nt(u32x4& dst, unsigned voff, const char* base) {
  asm volatile("global_load_dwordx4 %0, %1, %2 offset:%3 nt" : "=v"(dst) : "v"(voff), "s"(base), "i"(OFF));
}
__device__ __forceinline__ void ld_x4(u32x4& dst, unsigned voff, u32x4 srd, unsigned soff) {
  asm volatile("buffer_load_dwordx4 %0, %1, %2, %3 offen nt" : "=v"(dst) : "v"(voff), "s"(srd), "s"(soff));
}
template <int OFF> __device__ __forceinline__ void ld_x2(u32x2& dst, unsigned voff, u32x4 srd, unsigned soff) {
  asm volatile("buffer_load_dwordx2 %0, %1, %2, %3 offen offset:%4" : "=v"(dst) : "v"(voff), "s"(srd), "s"(soff), "i"(OFF));
}
__device__ __forceinline__ void st_rec(u32x4 v, unsigned voff, u32x4 srd, unsigned soff) {
  asm volatile("buffer_store_dwordx4 %0, %1, %2, %3 offen sc1\n\ts_nop 1" :: "v"(v), "v"(voff), "s"(srd), "s"(soff) : "memory");
}
__device__ __forceinline__ void ld_rec(u32x4& dst, unsigned voff, u32x4 srd, unsigned soff) {
  asm volatile("buffer_load_dwordx4 %0, %1, %2, %3 offen sc1" : "+v"(dst) : "v"(voff), "s"(srd), "s"(soff) : "memory");
}
#define WAIT2(N, a, b) asm volatile("s_waitcnt vmcnt(" #N ")" : "+v"(a), "+v"(b))
#define WAIT4(N, a, b, c, d) asm volatile("s_waitcnt vmcnt(" #N ")" : "+v"(a), "+v"(b), "+v"(c), "+v"(d))
#define WAITN2(N, a, b) asm volatile("s_waitcnt vmcnt(%2)" : "+v"(a), "+v"(b) : "i"(N))
#define WAITN4(N, a, b, c, d) asm volatile("s_waitcnt vmcnt(%4)" : "+v"(a), "+v"(b), "+v"(c), "+v"(d) : "i"(N))
#define PIN(x) asm volatile("" : "+v"(x))

constexpr int REC = 16 + GQ * D;

__global__ void __launch_bounds__(NTHR, 2) attn_decode(const unsigned short* __restrict__ q, const int* __restrict__ qk,
                                                               const int* __restrict__ qv, const unsigned short* __restrict__ ks,
                                                               const unsigned short* __restrict__ kz, const unsigned short* __restrict__ vs,
                                                               const unsigned short* __restrict__ vz, unsigned char* __restrict__ ws,
                                                               unsigned short* __restrict__ out) {
  unsigned long long d0; asm volatile("s_memrealtime %0\n\ts_waitcnt lgkmcnt(0)" : "=s"(d0)); __builtin_amdgcn_sched_barrier(0);
  __shared__ __attribute__((aligned(16))) float lds[WAVES * REC + WAVES * 132];
  const int tid = threadIdx.x, lane = tid & 63;
  const int wave = __builtin_amdgcn_readfirstlane(tid >> 6);
  const int bid = blockIdx.x;
  const int hk = bid / NSPLIT, sp = bid % NSPLIT;
  const int grp = sp * TOK_WG / GTOK;
  const int g = lane >> 4, hq = (lane >> 2) & 3, cq = lane & 3;
  const bool c1 = (lane & 1) != 0, c2 = (lane & 2) != 0;

  u32x2 qr0, qr1, ks0, ks1, kz0, kz1;
  u32x4 ctl;
  {
    const unsigned dq2 = (16 * g + 4 * cq) * 2;
    const u32x4 sq = make_srd(q, HQ * D * 2), sks = make_srd(ks, HKV * NGK * D * 2), skz = make_srd(kz, HKV * NGK * D * 2);
    const unsigned zero = 0;
    asm volatile("buffer_load_dwordx4 %0, %1, %2, %3 offen sc1" : "=v"(ctl) : "v"(zero), "s"(make_srd(ws, CTL_BYTES)), "s"(hk * 128));
    ld_x2<0>(qr0, dq2 + hq * (D * 2), sq, hk * (GQ * D * 2));   ld_x2<128>(qr1, dq2 + hq * (D * 2), sq, hk * (GQ * D * 2));
    ld_x2<0>(ks0, dq2, sks, (hk * NGK + grp) * (D * 2));        ld_x2<128>(ks1, dq2, sks, (hk * NGK + grp) * (D * 2));
    ld_x2<0>(kz0, dq2, skz, (hk * NGK + grp) * (D * 2));        ld_x2<128>(kz1, dq2, skz, (hk * NGK + grp) * (D * 2));
  }
  u32x4 sk = make_srd(qk, HKV * SKV * D * 4u), sv = make_srd(qv, HKV * SKV * D * 4u);
  u32x4 svs = make_srd(vs, HKV * SKV * 2u), svz = make_srd(vz, HKV * SKV * 2u);
  const unsigned kofs = hq * (8 * D * 4) + g * 64 + cq * 16, vofs = g * (8 * D * 4) + (lane & 15) * 16, sofs = g * 16;
  unsigned row = hk * SKV + sp * TOK_WG + wave * (MAPV ? TILE : TILE * NTILE);
  u32x4 K[16], V[16], S, Z;
#define KLD(j) do { ld_nt<(j) * 512>(K[2 * (j)], kofs, sk, row * (D * 4)); ld_nt<(j) * 512 + 256>(K[2 * (j) + 1], kofs, sk, row * (D * 4)); } while (0)
#define VLD(j) do { ld_nt<(j) * 512>(V[2 * (j)], vofs, sv, row * (D * 4)); ld_nt<(j) * 512 + 256>(V[2 * (j) + 1], vofs, sv, row * (D * 4)); } while (0)
  KLD(0); KLD(1); KLD(2); KLD(3); KLD(4); KLD(5); KLD(6); KLD(7);
  ld_x4(S, sofs, svs, row * 2); ld_x4(Z, sofs, svz, row * 2);
  VLD(0); VLD(1); VLD(2); VLD(3); VLD(4); VLD(5); VLD(6); VLD(7);
  __builtin_amdgcn_sched_barrier(0);
  asm volatile("s_waitcnt vmcnt(34)" : "+v"(ctl), "+v"(qr0), "+v"(qr1), "+v"(ks0), "+v"(ks1), "+v"(kz0), "+v"(kz1));
  const u32x4 srec = make_srd(ws + CTL_BYTES, HQ * NSPLIT * RECB);
  const unsigned myrec = ((hk * GQ + (wave & 3)) * NSPLIT + sp) * RECB;
  if (wave < GQ) {
    const u32x4 z4 = {0u, 0u, 0u, 0u};
    st_rec(z4, lane * 16, srec, myrec);
    if (lane == 0) st_rec(z4, 1024, srec, myrec);
  }
  unsigned tagw = 0, tagf = 0;
#pragma unroll
  for (int i = 0; i < GQ; ++i) {
    const unsigned t = (rfl(ctl[i]) * 1664525u + 1013904221u) | 1u;
    if (wave == i) tagw = t;
    if (sp == i) tagf = t;
  }
  constexpr float C2 = 0.088388347648318440f * 1.4426950408889634f;
  u32x4 Qh, Ql; float bias = 0.f;
  {
    const unsigned qq[4] = {qr0[0], qr0[1], qr1[0], qr1[1]}, kk[4] = {ks0[0], ks0[1], ks1[0], ks1[1]}, zz[4] = {kz0[0], kz0[1], kz1[0], kz1[1]};
#pragma unroll
    for (int w = 0; w < 4; ++w) {
      const float a0 = bf_lo(qq[w]) * bf_lo(kk[w]) * C2, a1 = bf_hi(qq[w]) * bf_hi(kk[w]) * C2;
      const unsigned hi = pk_bf16(a0, a1);
      const unsigned lo = pk_bf16(a0 - bf_lo(hi), a1 - bf_hi(hi));
      Qh[w] = hi; Ql[w] = lo;
      bias = fmaf(-(bf_lo(hi) + bf_lo(lo)), bf_lo(zz[w]), bias);
      bias = fmaf(-(bf_hi(hi) + bf_hi(lo)), bf_hi(zz[w]), bias);
    }
  }
  bias += dpp<XOR1>(bias); bias += dpp<XOR2>(bias); bias = x32_sum(x16_sum(bias));

  float m = -INFINITY, lsum = 0.f, zsum = 0.f;
  f32x4 acc[8];
#pragma unroll
  for (int i = 0; i < 8; ++i) acc[i] = f32x4{0.f, 0.f, 0.f, 0.f};

  auto tile = [&](auto last_tag) __attribute__((always_inline)) {
    constexpr bool LAST = decltype(last_tag)::value;
    float s[8];
#define QK(j) do { \
      WAITN2(LAST ? 32 - 2 * (j) : 32, K[2 * (j)], K[2 * (j) + 1]); \
      u32x4 a = {pk_int(K[2 * (j)][0], K[2 * (j)][1]), pk_int(K[2 * (j)][2], K[2 * (j)][3]), \
                 pk_int(K[2 * (j) + 1][0], K[2 * (j) + 1][1]), pk_int(K[2 * (j) + 1][2], K[2 * (j) + 1][3])}; \
      PIN(a); \
      if (!LAST) KLD(j); \
      f32x4 d = mfma16(a, Qh, f32x4{0.f, 0.f, 0.f, 0.f}); \
      d = mfma16(a, Ql, d); \
      const float x0 = c1 ? d[1] : d[0], x1 = c1 ? d[3] : d[2]; \
      float x = c2 ? x1 : x0; \
      x += dpp<XOR1>(x); x += dpp<XOR2>(x); \
      s[j] = x; } while (0)
    QK(0); QK(1); QK(2); QK(3); QK(4); QK(5); QK(6); QK(7);
    float tm = fmaxf(fmaxf(fmaxf(s[0], s[1]), fmaxf(s[2], s[3])), fmaxf(fmaxf(s[4], s[5]), fmaxf(s[6], s[7])));
    tm = x32_max(x16_max(tm));
    const float mn = fmaxf(m, tm);
    const float alpha = __builtin_amdgcn_exp2f(m - mn);
    m = mn;
    WAITN2(LAST ? 16 : 32, S, Z);
    float w[8];
    float ls = 0.f, zs = 0.f;
#pragma unroll
    for (int j = 0; j < 8; ++j) {
      const float p = __builtin_amdgcn_exp2f(s[j] - mn);
      const float sc = (j & 1) ? bf_hi(S[j >> 1]) : bf_lo(S[j >> 1]);
      const float zz = (j & 1) ? bf_hi(Z[j >> 1]) : bf_lo(Z[j >> 1]);
      w[j] = p * sc; ls += p; zs = fmaf(w[j], zz, zs);
    }
    lsum = fmaf(lsum, alpha, ls); zsum = fmaf(zsum, alpha, zs);
    u32x4 P;
#pragma unroll
    for (int j = 0; j < 4; ++j) {
      const unsigned hi = pk_bf16(w[2 * j], w[2 * j + 1]);
      const unsigned lo = pk_bf16(w[2 * j] - bf_lo(hi), w[2 * j + 1] - bf_hi(hi));
      P[j] = c1 ? lo : hi;
    }
    asm volatile("" : "+v"(P), "+v"(zsum));
    if (!LAST) { ld_x4(S, sofs, svs, row * 2); ld_x4(Z, sofs, svz, row * 2); }
    {
      int ai = __builtin_bit_cast(int, alpha), ag = ai;
      ag = __builtin_amdgcn_update_dpp(ag, ai, 0x150, 0x1, 0xf, false);
      ag = __builtin_amdgcn_update_dpp(ag, ai, 0x154, 0x2, 0xf, false);
      ag = __builtin_amdgcn_update_dpp(ag, ai, 0x158, 0x4, 0xf, false);
      ag = __builtin_amdgcn_update_dpp(ag, ai, 0x15C, 0x8, 0xf, false);
      const float af = __builtin_bit_cast(float, ag);
#pragma unroll
      for (int i = 0; i < 8; ++i) { acc[i][0] *= af; acc[i][1] *= af; }
    }
    u32x4 B[8];
#define PVC(jp) do { \
      WAITN4(LAST ? 12 - 4 * (jp) : 30, V[4 * (jp)], V[4 * (jp) + 1], V[4 * (jp) + 2], V[4 * (jp) + 3]); \
      unsigned b0 = pk_int(V[4 * (jp)][0], V[4 * (jp) + 2][0]), b1 = pk_int(V[4 * (jp)][1], V[4 * (jp) + 2][1]), \
               b2 = pk_int(V[4 * (jp)][2], V[4 * (jp) + 2][2]), b3 = pk_int(V[4 * (jp)][3], V[4 * (jp) + 2][3]), \
               b4 = pk_int(V[4 * (jp) + 1][0], V[4 * (jp) + 3][0]), b5 = pk_int(V[4 * (jp) + 1][1], V[4 * (jp) + 3][1]), \
               b6 = pk_int(V[4 * (jp) + 1][2], V[4 * (jp) + 3][2]), b7 = pk_int(V[4 * (jp) + 1][3], V[4 * (jp) + 3][3]); \
      asm volatile("" : "+v"(b0), "+v"(b1), "+v"(b2), "+v"(b3), "+v"(b4), "+v"(b5), "+v"(b6), "+v"(b7)); \
      if (!LAST) { VLD(2 * (jp)); VLD(2 * (jp) + 1); } \
      B[0][jp] = b0; B[1][jp] = b1; B[2][jp] = b2; B[3][jp] = b3; B[4][jp] = b4; B[5][jp] = b5; B[6][jp] = b6; B[7][jp] = b7; } while (0)
    PVC(0); PVC(1); PVC(2); PVC(3);
#pragma unroll
    for (int i = 0; i < 8; ++i) acc[i] = mfma16(P, B[i], acc[i]);
  };
#pragma unroll 1
  for (int it = 0; it + 1 < NTILE; ++it) {
    row += MAPV ? WAVES * TILE : TILE;
    tile(std::false_type{});
  }
  tile(std::true_type{});

  asm volatile("" :: "v"(acc[0]), "v"(acc[7]));
  const unsigned long long d2 = __builtin_amdgcn_s_memrealtime();
  lsum = x32_sum(x16_sum(lsum)); zsum = x32_sum(x16_sum(zsum));
  float* rec = lds + wave * REC;
  if (lane < 16 && cq == 0) { rec[hq] = m + bias; rec[4 + hq] = lsum; rec[8 + hq] = zsum; }
#pragma unroll
  for (int h = 0; h < 2; ++h) {
    f32x4 o;
#pragma unroll
    for (int e = 0; e < 4; ++e) o[e] = acc[4 * h + e][0] + acc[4 * h + e][1];
    *reinterpret_cast<f32x4*>(rec + 16 + g * D + 64 * h + 4 * (lane & 15)) = o;
  }
  __syncthreads();
  if (wave < GQ) {
    const int h = wave;
    float M = -INFINITY;
#pragma unroll
    for (int wv = 0; wv < WAVES; ++wv) M = fmaxf(M, lds[wv * REC + h]);
    float L = 0.f, o0 = 0.f, o1 = 0.f;
#pragma unroll
    for (int wv = 0; wv < WAVES; ++wv) {
      const float f = __builtin_amdgcn_exp2f(lds[wv * REC + h] - M), fz = -f * lds[wv * REC + 8 + h];
      const f32x2 o = *reinterpret_cast<const f32x2*>(lds + wv * REC + 16 + h * D + 2 * lane);
      L = fmaf(f, lds[wv * REC + 4 + h], L);
      o0 += fmaf(f, o[0], fz); o1 += fmaf(f, o[1], fz);
    }
    st_rec(u32x4{__float_as_uint(o0), tagw, __float_as_uint(o1), tagw}, lane * 16, srec, myrec);
    if (lane == 0) st_rec(u32x4{__float_as_uint(M), tagw, __float_as_uint(L), tagw}, 1024, srec, myrec);
    if (lane == 0) st_rec(u32x4{(unsigned)d0, tagw, (unsigned)__builtin_amdgcn_s_memrealtime(), tagw}, 1040, srec, myrec);
  }
  if (sp < GQ) {
    const unsigned rb = ((hk * GQ + sp) * NSPLIT + wave * RPW) * RECB;
    u32x4 R[RPW], ML = {0u, 0u, 0u, 0u};
#pragma unroll
    for (int r = 0; r < RPW; ++r) R[r] = u32x4{0u, 0u, 0u, 0u};
    unsigned pend = (1u << RPW) - 1u;
    for (unsigned spins = 0; spins < (1u << 20); ++spins) {
      ld_rec(ML, (lane & (RPW - 1)) * RECB + 1024, srec, rb);
#pragma unroll
      for (int r = 0; r < RPW; ++r) if (pend & (1u << r)) ld_rec(R[r], lane * 16, srec, rb + r * RECB);
      asm volatile("s_waitcnt vmcnt(0)" : "+v"(ML));
#pragma unroll
      for (int r = 0; r < RPW; ++r) { PIN(R[r]); if (__all(R[r][1] == tagf && R[r][3] == tagf)) pend &= ~(1u << r); }
      if (pend == 0 && __all(ML[1] == tagf && ML[3] == tagf)) break;
      __builtin_amdgcn_s_sleep(2);
    }
    const float mi = __uint_as_float(ML[0]), li = __uint_as_float(ML[2]);
    float M = mi;
    M = fmaxf(M, dpp<XOR1>(M)); M = fmaxf(M, dpp<XOR2>(M));
    if (RPW == 8) M = fmaxf(M, dpp<HMIRROR>(M));
    const float fi = __builtin_amdgcn_exp2f(mi - M);
    float L = fi * li;
    L += dpp<XOR1>(L); L += dpp<XOR2>(L);
    if (RPW == 8) L += dpp<HMIRROR>(L);
    float o0 = 0.f, o1 = 0.f;
#pragma unroll
    for (int r = 0; r < RPW; ++r) {
      const float f = __builtin_bit_cast(float, __builtin_amdgcn_readlane(__builtin_bit_cast(int, fi), r));
      o0 = fmaf(f, __uint_as_float(R[r][0]), o0); o1 = fmaf(f, __uint_as_float(R[r][2]), o1);
    }
    float* fin = lds + WAVES * REC;
    if (lane == 0) { fin[wave * 132] = M; fin[wave * 132 + 1] = L; }
    *reinterpret_cast<f32x2*>(fin + wave * 132 + 4 + 2 * lane) = f32x2{o0, o1};
    __syncthreads();
    if (wave == 0) {
      float MM = -INFINITY;
#pragma unroll
      for (int wv = 0; wv < WAVES; ++wv) MM = fmaxf(MM, fin[wv * 132]);
      float LL = 0.f, a0 = 0.f, a1 = 0.f;
#pragma unroll
      for (int wv = 0; wv < WAVES; ++wv) {
        const float f = __builtin_amdgcn_exp2f(fin[wv * 132] - MM);
        const f32x2 o = *reinterpret_cast<const f32x2*>(fin + wv * 132 + 4 + 2 * lane);
        LL = fmaf(f, fin[wv * 132 + 1], LL); a0 = fmaf(f, o[0], a0); a1 = fmaf(f, o[1], a1);
      }
      const float inv = 1.f / LL;
      *reinterpret_cast<unsigned*>(out + (hk * GQ + sp) * D + 2 * lane) = pk_bf16(a0 * inv, a1 * inv);
      { u32x4 T = {0u, 0u, 0u, 0u};
        for (int k = 0; k < 1000; ++k) { ld_rec(T, (lane & 31) * RECB + 1040, srec, ((hk * GQ + sp) * NSPLIT) * RECB); asm volatile("s_waitcnt vmcnt(0)" : "+v"(T)); if (__all(T[1] == tagf && T[3] == tagf)) break; }
        int rel = (int)(T[0] - (unsigned)d0), relp = (int)(T[2] - (unsigned)d0);
        int mn = rel, mx = rel, pmn = relp, pmx = relp;
        for (int o = 1; o < 32; o <<= 1) { mn = min(mn, __shfl_xor(mn, o)); mx = max(mx, __shfl_xor(mx, o)); pmn = min(pmn, __shfl_xor(pmn, o)); pmx = max(pmx, __shfl_xor(pmx, o)); }
        mn = __builtin_amdgcn_readfirstlane(mn); mx = __builtin_amdgcn_readfirstlane(mx); pmn = __builtin_amdgcn_readfirstlane(pmn); pmx = __builtin_amdgcn_readfirstlane(pmx);
        const unsigned long long d3 = __builtin_amdgcn_s_memrealtime();
        long long dl = (0); dl = dl < 0 ? 0 : (dl > 20000 ? 20000 : dl);
        const unsigned long long until = d3 + (unsigned long long)dl;
        while (__builtin_amdgcn_s_memrealtime() < until) __builtin_amdgcn_s_sleep(1); }
      if (lane == 0) __hip_atomic_store((gu32*)(ws + hk * 128 + sp * 4), tagf, __ATOMIC_RELAXED, __HIP_MEMORY_SCOPE_AGENT);
    }
  }
}

}

extern "C" void kernel_launch(void* const* d_in, const int* in_sizes, int n_in, void* d_out, int out_size, void* d_ws,
                              size_t ws_size, hipStream_t stream) {
  attn_decode<<<HKV * NSPLIT, NTHR, 0, stream>>>((const unsigned short*)d_in[0], (const int*)d_in[1], (const int*)d_in[2],
                                                 (const unsigned short*)d_in[3], (const unsigned short*)d_in[4],
                                                 (const unsigned short*)d_in[5], (const unsigned short*)d_in[6],
                                                 (unsigned char*)d_ws, (unsigned short*)d_out);
}
```

```cpp
#include <hip/hip_runtime.h>
#include <hip/hip_bf16.h>
#include <stdint.h>
#include <type_traits>

namespace {

constexpr int HQ = 32, HKV = 8, GQ = HQ / HKV, SKV = 32768, D = 128, NGK = 2, GTOK = SKV / NGK;
constexpr int WAVES = 4, NTHR = WAVES * 64;
constexpr int NSPLIT = 32;
constexpr int TOK_WG = SKV / NSPLIT;
constexpr int WG_GRP = NSPLIT / NGK;
constexpr int TILE = 32;
constexpr int NTILE = TOK_WG / (WAVES * TILE);
static_assert(GQ == 4 && TOK_WG % (WAVES * TILE) == 0 && GTOK % TOK_WG == 0, "shape");

constexpr int CTL_BYTES = 4096;
constexpr int RECB = 1024 + 128;
constexpr int TAU = 3505;
constexpr int TOFF = 260;
constexpr int COLD0 = 55, COLD_TAU = 12, COLD_OFF = 12;
constexpr int GDELAY = 27;
constexpr unsigned GIVE_UP = 20000000u;

typedef unsigned u32x4 __attribute__((ext_vector_type(4)));
typedef unsigned u32x2 __attribute__((ext_vector_type(2)));
typedef float  f32x4 __attribute__((ext_vector_type(4)));
typedef float  f32x2 __attribute__((ext_vector_type(2)));
typedef __bf16 bf16x8 __attribute__((ext_vector_type(8)));
typedef __bf16 bf16x2 __attribute__((ext_vector_type(2)));
typedef __attribute__((address_space(1))) unsigned gu32;
typedef __attribute__((address_space(3))) volatile int las_int;

__device__ __forceinline__ unsigned pk_bf16(float lo, float hi) {
  f32x2 f = {lo, hi};
  bf16x2 b = __builtin_convertvector(f, bf16x2);
  return __builtin_bit_cast(unsigned, b);
}
__device__ __forceinline__ unsigned pk_int(unsigned lo, unsigned hi) { return pk_bf16((float)(int)lo, (float)(int)hi); }
__device__ __forceinline__ float bf_lo(unsigned u) { return __builtin_bit_cast(float, u << 16); }
__device__ __forceinline__ float bf_hi(unsigned u) { return __builtin_bit_cast(float, u & 0xffff0000u); }
template <int CTRL> __device__ __forceinline__ float dpp(float x) {
  return __builtin_bit_cast(float, __builtin_amdgcn_mov_dpp(__builtin_bit_cast(int, x), CTRL, 0xf, 0xf, true));
}
constexpr int XOR1 = 0xB1, XOR2 = 0x4E, HMIRROR = 0x141, MIRROR = 0x140;
__device__ __forceinline__ float x16_max(float x) {
  auto s = __builtin_amdgcn_permlane16_swap(__float_as_uint(x), __float_as_uint(x), false, false);
  return fmaxf(__uint_as_float(s[0]), __uint_as_float(s[1]));
}
__device__ __forceinline__ float x32_max(float x) {
  auto t = __builtin_amdgcn_permlane32_swap(__float_as_uint(x), __float_as_uint(x), false, false);
  return fmaxf(__uint_as_float(t[0]), __uint_as_float(t[1]));
}
__device__ __forceinline__ float x16_sum(float x) {
  auto s = __builtin_amdgcn_permlane16_swap(__float_as_uint(x), __float_as_uint(x), false, false);
  return __uint_as_float(s[0]) + __uint_as_float(s[1]);
}
__device__ __forceinline__ float x32_sum(float x) {
  auto t = __builtin_amdgcn_permlane32_swap(__float_as_uint(x), __float_as_uint(x), false, false);
  return __uint_as_float(t[0]) + __uint_as_float(t[1]);
}
__device__ __forceinline__ float to_rows(float x) {
  const int xi = __builtin_bit_cast(int, x); int r = xi;
  r = __builtin_amdgcn_update_dpp(r, xi, 0x150, 0x1, 0xf, false);
  r = __builtin_amdgcn_update_dpp(r, xi, 0x154, 0x2, 0xf, false);
  r = __builtin_amdgcn_update_dpp(r, xi, 0x158, 0x4, 0xf, false);
  r = __builtin_amdgcn_update_dpp(r, xi, 0x15C, 0x8, 0xf, false);
  return __builtin_bit_cast(float, r);
}
__device__ __forceinline__ unsigned rfl(unsigned x) { return (unsigned)__builtin_amdgcn_readfirstlane((int)x); }
__device__ __forceinline__ unsigned now32() { return (unsigned)__builtin_amdgcn_s_memrealtime(); }
__device__ __forceinline__ f32x4 mfma16(u32x4 a, u32x4 b, f32x4 c) {
  return __builtin_amdgcn_mfma_f32_16x16x32_bf16(__builtin_bit_cast(bf16x8, a), __builtin_bit_cast(bf16x8, b), c, 0, 0, 0);
}
__device__ __forceinline__ u32x4 make_srd(const void* p, unsigned bytes) {
  return u32x4{(unsigned)(size_t)p, (unsigned)((size_t)p >> 32) & 0xffffu, bytes, 0x00020000u};
}
template <int OFF> __device__ __forceinline__ void ld_nt(u32x4& dst, unsigned voff, u32x4 srd, unsigned soff) {
  asm volatile("buffer_load_dwordx4 %0, %1, %2, %3 offen offset:%4 nt" : "=v"(dst) : "v"(voff), "s"(srd), "s"(soff), "i"(OFF));
}
template <int OFF> __device__ __forceinline__ void ld_x2(u32x2& dst, unsigned voff, u32x4 srd, unsigned soff) {
  asm volatile("buffer_load_dwordx2 %0, %1, %2, %3 offen offset:%4" : "=v"(dst) : "v"(voff), "s"(srd), "s"(soff), "i"(OFF));
}
template <int OFF> __device__ __forceinline__ void st_rec(u32x4 v, unsigned voff, u32x4 srd, unsigned soff) {
  asm volatile("buffer_store_dwordx4 %0, %1, %2, %3 offen offset:%4 sc1\n\ts_nop 1" :: "v"(v), "v"(voff), "s"(srd), "s"(soff), "i"(OFF) : "memory");
}
template <int OFF> __device__ __forceinline__ void ld_rec(u32x4& dst, unsigned voff, u32x4 srd, unsigned soff) {
  asm volatile("buffer_load_dwordx4 %0, %1, %2, %3 offen offset:%4 sc1" : "=v"(dst) : "v"(voff), "s"(srd), "s"(soff), "i"(OFF) : "memory");
}
#define WAIT1(N, a) asm volatile("s_waitcnt vmcnt(%1)" : "+v"(a) : "i"(N))
#define WAIT2(N, a, b) asm volatile("s_waitcnt vmcnt(%2)" : "+v"(a), "+v"(b) : "i"(N))
#define WAIT4(N, a, b, c, d) asm volatile("s_waitcnt vmcnt(%4)" : "+v"(a), "+v"(b), "+v"(c), "+v"(d) : "i"(N))
#define PIN(x) asm volatile("" : "+v"(x))

template <int R> struct Gather {
  static __device__ __forceinline__ void issue(u32x4* G, unsigned voff, u32x4 srd, const unsigned* rb) {
    ld_rec<(R & 3) * RECB>(G[R], voff, srd, rb[R >> 2]);
    if constexpr (R + 1 < NSPLIT) Gather<R + 1>::issue(G, voff, srd, rb);
  }
  static __device__ __forceinline__ void sum(u32x4* G, float f, unsigned tag, unsigned& bad, float& o0, float& o1) {
    WAIT1(NSPLIT - 1 - R, G[R]);
    const float fr = __builtin_bit_cast(float, __builtin_amdgcn_readlane(__builtin_bit_cast(int, f), R));
    bad |= (G[R][1] ^ tag) | (G[R][3] ^ tag);
    o0 = fmaf(fr, __uint_as_float(G[R][0]), o0); o1 = fmaf(fr, __uint_as_float(G[R][2]), o1);
    asm volatile("" : "+v"(bad), "+v"(o0), "+v"(o1));
    if constexpr (R + 1 < NSPLIT) Gather<R + 1>::sum(G, f, tag, bad, o0, o1);
  }
};

__global__ void __launch_bounds__(NTHR, 2) attn_decode(const unsigned short* __restrict__ q, const int* __restrict__ qk,
                                                       const int* __restrict__ qv, const unsigned short* __restrict__ ks,
                                                       const unsigned short* __restrict__ kz, const unsigned short* __restrict__ vs,
                                                       const unsigned short* __restrict__ vz, unsigned char* __restrict__ ws,
                                                       unsigned short* __restrict__ out) {
  unsigned long long tentry, tstart;
  asm volatile("s_memrealtime %0" : "=s"(tentry));
  __builtin_amdgcn_sched_barrier(0);
  asm volatile("s_memrealtime %0" : "=s"(tstart)
               : "s"(q), "s"(qk), "s"(qv), "s"(ks), "s"(kz), "s"(vs), "s"(vz), "s"(ws), "s"(out));
  __builtin_amdgcn_sched_barrier(0);
  __shared__ __attribute__((aligned(16))) float lds[3 * 768 + 4 + WAVES * 256];
  las_int* const flag = (las_int*)(lds + 3 * 768);
  const int tid = threadIdx.x, lane = tid & 63;
  const int wave = __builtin_amdgcn_readfirstlane(tid >> 6);
  const int bid = blockIdx.x;
  const int hk = bid % HKV, sp = bid / HKV;
  const int fq = sp - (NSPLIT - GQ);
  const int grp = sp / WG_GRP;
  const int g = lane >> 4, hq = (lane >> 2) & 3, cq = lane & 3, col = lane & 15;
  const bool c1 = (lane & 1) != 0, c2 = (lane & 2) != 0;

  u32x2 qr0, qr1, ks0, ks1, kz0, kz1;
  u32x4 ctl;
  {
    const unsigned dq2 = (16 * g + 4 * cq) * 2;
    const u32x4 sq = make_srd(q, HQ * D * 2), sks = make_srd(ks, HKV * NGK * D * 2), skz = make_srd(kz, HKV * NGK * D * 2);
    ld_rec<0>(ctl, 0u, make_srd(ws, (wave == WAVES - 1 || (wave == 0 && fq >= 0)) ? CTL_BYTES : 0), hk * 128);
    ld_x2<0>(qr0, dq2 + hq * (D * 2), sq, hk * (GQ * D * 2));   ld_x2<128>(qr1, dq2 + hq * (D * 2), sq, hk * (GQ * D * 2));
    ld_x2<0>(ks0, dq2, sks, (hk * NGK + grp) * (D * 2));        ld_x2<128>(ks1, dq2, sks, (hk * NGK + grp) * (D * 2));
    ld_x2<0>(kz0, dq2, skz, (hk * NGK + grp) * (D * 2));        ld_x2<128>(kz1, dq2, skz, (hk * NGK + grp) * (D * 2));
  }
  const u32x4 sk = make_srd(qk, HKV * SKV * D * 4u), sv = make_srd(qv, HKV * SKV * D * 4u);
  const u32x4 svs = make_srd(vs, HKV * SKV * 2u), svz = make_srd(vz, HKV * SKV * 2u);
  const unsigned kofs = hq * (8 * D * 4) + g * 64 + cq * 16, vofs = g * (8 * D * 4) + col * 16;
  unsigned row = hk * SKV + grp * GTOK + (wave * WG_GRP + sp % WG_GRP) * TILE;
  u32x4 K[16], V[16], S, Z;
  const unsigned sofs = ((lane >> 2) & 7) * (WG_GRP * WAVES * TILE * 2) + (lane & 3) * 16;
  ld_nt<0>(S, sofs, svs, row * 2); ld_nt<0>(Z, sofs, svz, row * 2);
#define KLD(j) do { ld_nt<(j) * 512>(K[2 * (j)], kofs, sk, row * (D * 4)); ld_nt<(j) * 512 + 256>(K[2 * (j) + 1], kofs, sk, row * (D * 4)); } while (0)
#define VLD(j) do { ld_nt<(j) * 512>(V[2 * (j)], vofs, sv, row * (D * 4)); ld_nt<(j) * 512 + 256>(V[2 * (j) + 1], vofs, sv, row * (D * 4)); } while (0)
  KLD(0); KLD(1); KLD(2); KLD(3); KLD(4); KLD(5); KLD(6); KLD(7);
  __builtin_amdgcn_sched_barrier(0);

  if (tid < 4) flag[tid] = 0;
  asm volatile("s_waitcnt lgkmcnt(0)" : "+s"(tentry), "+s"(tstart) :: "memory");
  __builtin_amdgcn_s_barrier();

  const unsigned cold = min(max((int)(unsigned)(tstart - tentry) - COLD0, 0), 32);
  const unsigned STEP16 = (TAU + cold * COLD_TAU / 32) * 16 / (2 * NTILE);
  const unsigned wslot = wave * (HKV * NSPLIT) + bid;
  unsigned due16 = ((unsigned)tstart + TOFF + cold * COLD_OFF / 32) * 16u + wslot * STEP16 / (WAVES * HKV * NSPLIT);
#define PACE() do { while ((int)(now32() * 16u - due16) < 0) __builtin_amdgcn_s_sleep(1); due16 += STEP16; } while (0)

  PACE();
  VLD(0); VLD(1); VLD(2); VLD(3); VLD(4); VLD(5); VLD(6); VLD(7);
  __builtin_amdgcn_sched_barrier(0);

  asm volatile("s_waitcnt vmcnt(32)" : "+v"(ctl), "+v"(qr0), "+v"(qr1), "+v"(ks0), "+v"(ks1), "+v"(kz0), "+v"(kz1), "+v"(S), "+v"(Z));
  u32x4* szp = reinterpret_cast<u32x4*>(lds + 3 * 768 + 4 + wave * 256);
  szp[lane] = lane < 32 ? S : Z;
  szp += g;
  unsigned tagr = 0, tagf = 0;
#pragma unroll
  for (int i = 0; i < GQ; ++i) {
    const unsigned t = (rfl(ctl[i]) * 1664525u + 1013904221u) | 1u;
    tagr = g == i ? t : tagr;
    if (fq == i) tagf = t;
  }
  asm volatile("" : "+v"(tagr), "+s"(tagf));
  const u32x4 srec = make_srd(ws + CTL_BYTES, HQ * NSPLIT * RECB);
  const unsigned recv = g * (NSPLIT * RECB) + col * 16, recs = (hk * GQ * NSPLIT + sp) * RECB;

  constexpr float C2 = 0.088388347648318440f * 1.4426950408889634f;
  u32x4 Qh, Ql; float bias = 0.f;
  {
    const unsigned qq[4] = {qr0[0], qr0[1], qr1[0], qr1[1]}, kk[4] = {ks0[0], ks0[1], ks1[0], ks1[1]}, zz[4] = {kz0[0], kz0[1], kz1[0], kz1[1]};
#pragma unroll
    for (int w = 0; w < 4; ++w) {
      const float a0 = bf_lo(qq[w]) * bf_lo(kk[w]) * C2, a1 = bf_hi(qq[w]) * bf_hi(kk[w]) * C2;
      const unsigned hi = pk_bf16(a0, a1);
      const unsigned lo = pk_bf16(a0 - bf_lo(hi), a1 - bf_hi(hi));
      Qh[w] = hi; Ql[w] = lo;
      bias = fmaf(-(bf_lo(hi) + bf_lo(lo)), bf_lo(zz[w]), bias);
      bias = fmaf(-(bf_hi(hi) + bf_hi(lo)), bf_hi(zz[w]), bias);
    }
  }
  bias += dpp<XOR1>(bias); bias += dpp<XOR2>(bias); bias = x32_sum(x16_sum(bias));

  float m = -INFINITY, lsum = 0.f, zsum = 0.f, M, L, zr;
  f32x4 acc[8];
#pragma unroll
  for (int i = 0; i < 8; ++i) acc[i] = f32x4{0.f, 0.f, 0.f, 0.f};

  u32x4 P;
  auto qks = [&]() __attribute__((always_inline)) {
    float s[8];
#define QK(j) do { \
      WAIT2(30 - 2 * (j), K[2 * (j)], K[2 * (j) + 1]); \
      u32x4 a = {pk_int(K[2 * (j)][0], K[2 * (j)][1]), pk_int(K[2 * (j)][2], K[2 * (j)][3]), \
                 pk_int(K[2 * (j) + 1][0], K[2 * (j) + 1][1]), pk_int(K[2 * (j) + 1][2], K[2 * (j) + 1][3])}; \
      f32x4 d = mfma16(a, Qh, f32x4{0.f, 0.f, 0.f, 0.f}); \
      d = mfma16(a, Ql, d); \
      const float x0 = c1 ? d[1] : d[0], x1 = c1 ? d[3] : d[2]; \
      float x = c2 ? x1 : x0; \
      x += dpp<XOR1>(x); x += dpp<XOR2>(x); \
      s[j] = x; } while (0)
    QK(0); QK(1); QK(2); QK(3); QK(4); QK(5); QK(6); QK(7);
    float tm = fmaxf(fmaxf(fmaxf(s[0], s[1]), fmaxf(s[2], s[3])), fmaxf(fmaxf(s[4], s[5]), fmaxf(s[6], s[7])));
    tm = x32_max(x16_max(tm));
    const float mn = fmaxf(m, tm);
    const float alpha = __builtin_amdgcn_exp2f(m - mn);
    m = mn;
    S = szp[0]; Z = szp[32]; szp += 4;
    float w[8];
    float ls = 0.f, zs = 0.f;
#pragma unroll
    for (int j = 0; j < 8; ++j) {
      const float p = __builtin_amdgcn_exp2f(s[j] - mn);
      const float sc = (j & 1) ? bf_hi(S[j >> 1]) : bf_lo(S[j >> 1]);
      const float zz = (j & 1) ? bf_hi(Z[j >> 1]) : bf_lo(Z[j >> 1]);
      w[j] = p * sc; ls += p; zs = fmaf(w[j], zz, zs);
    }
    lsum = fmaf(lsum, alpha, ls); zsum = fmaf(zsum, alpha, zs);
#pragma unroll
    for (int j = 0; j < 4; ++j) {
      const unsigned hi = pk_bf16(w[2 * j], w[2 * j + 1]);
      const unsigned lo = pk_bf16(w[2 * j] - bf_lo(hi), w[2 * j + 1] - bf_hi(hi));
      P[j] = c1 ? lo : hi;
    }
    const float af = to_rows(alpha);
#pragma unroll
    for (int i = 0; i < 8; ++i) { acc[i][0] *= af; acc[i][1] *= af; }
    asm volatile("" : "+v"(P), "+v"(zsum), "+v"(lsum), "+v"(m));
  };
  auto pv = [&](auto last_tag) __attribute__((always_inline)) {
    constexpr bool LAST = decltype(last_tag)::value;
    u32x4 B[8];
#define PVC(jp) do { \
      WAIT4((LAST ? 12 : 28) - 4 * (jp), V[4 * (jp)], V[4 * (jp) + 1], V[4 * (jp) + 2], V[4 * (jp) + 3]); \
      unsigned b0 = pk_int(V[4 * (jp)][0], V[4 * (jp) + 2][0]), b1 = pk_int(V[4 * (jp)][1], V[4 * (jp) + 2][1]), \
               b2 = pk_int(V[4 * (jp)][2], V[4 * (jp) + 2][2]), b3 = pk_int(V[4 * (jp)][3], V[4 * (jp) + 2][3]), \
               b4 = pk_int(V[4 * (jp) + 1][0], V[4 * (jp) + 3][0]), b5 = pk_int(V[4 * (jp) + 1][1], V[4 * (jp) + 3][1]), \
               b6 = pk_int(V[4 * (jp) + 1][2], V[4 * (jp) + 3][2]), b7 = pk_int(V[4 * (jp) + 1][3], V[4 * (jp) + 3][3]); \
      asm volatile("" : "+v"(b0), "+v"(b1), "+v"(b2), "+v"(b3), "+v"(b4), "+v"(b5), "+v"(b6), "+v"(b7)); \
      B[0][jp] = b0; B[1][jp] = b1; B[2][jp] = b2; B[3][jp] = b3; B[4][jp] = b4; B[5][jp] = b5; B[6][jp] = b6; B[7][jp] = b7; } while (0)
    PVC(0); PVC(1); PVC(2); PVC(3);
#pragma unroll
    for (int i = 0; i < 8; ++i) acc[i] = mfma16(P, B[i], acc[i]);
  };
  qks();
#pragma unroll 1
  for (int it = 0; it + 1 < NTILE; ++it) {
    row += WG_GRP * WAVES * TILE;
    PACE();
    KLD(0); KLD(1); KLD(2); KLD(3); KLD(4); KLD(5); KLD(6); KLD(7);
    pv(std::false_type{});
    PACE();
    VLD(0); VLD(1); VLD(2); VLD(3); VLD(4); VLD(5); VLD(6); VLD(7);
    qks();
  }
  M = to_rows(m + bias); L = to_rows(x32_sum(x16_sum(lsum)));
  zr = to_rows(x32_sum(x16_sum(zsum)));
  asm volatile("" : "+v"(M), "+v"(L), "+v"(zr));
  pv(std::true_type{});

  float o[8], fo, base[8];
  fo = 1.f;
#pragma unroll
  for (int i = 0; i < 8; ++i) base[i] = -zr;
  if (wave > 0) {
    while (flag[wave - 1] == 0) __builtin_amdgcn_s_sleep(1);
    asm volatile("" ::: "memory");
    const f32x4* src = reinterpret_cast<const f32x4*>(lds + (wave - 1) * 768) + lane;
    const f32x4 p0 = src[0], p1 = src[64], pm = src[128];
    const float Mn = fmaxf(M, pm[0]);
    fo = __builtin_amdgcn_exp2f(M - Mn);
    const float fp = __builtin_amdgcn_exp2f(pm[0] - Mn), fz = -fo * zr;
    M = Mn; L = fmaf(fo, L, fp * pm[1]);
#pragma unroll
    for (int e = 0; e < 4; ++e) { base[e] = fmaf(fp, p0[e], fz); base[4 + e] = fmaf(fp, p1[e], fz); }
  }
#pragma unroll
  for (int i = 0; i < 8; ++i) o[i] = fmaf(fo, acc[i][0] + acc[i][1], base[i]);
  if (wave < WAVES - 1) {
    f32x4* dst = reinterpret_cast<f32x4*>(lds + wave * 768) + lane;
    dst[0] = f32x4{o[0], o[1], o[2], o[3]}; dst[64] = f32x4{o[4], o[5], o[6], o[7]}; dst[128] = f32x4{M, L, 0.f, 0.f};
    asm volatile("s_waitcnt lgkmcnt(0)" ::: "memory");
    if (lane == 0) flag[wave] = 1;
  } else {
    if (col == 0) st_rec<1024>(u32x4{__float_as_uint(M), tagr, __float_as_uint(L), tagr}, recv, srec, recs);
    st_rec<0>(u32x4{__float_as_uint(o[0]), tagr, __float_as_uint(o[1]), tagr}, recv, srec, recs);
    st_rec<256>(u32x4{__float_as_uint(o[2]), tagr, __float_as_uint(o[3]), tagr}, recv, srec, recs);
    st_rec<512>(u32x4{__float_as_uint(o[4]), tagr, __float_as_uint(o[5]), tagr}, recv, srec, recs);
    st_rec<768>(u32x4{__float_as_uint(o[6]), tagr, __float_as_uint(o[7]), tagr}, recv, srec, recs);
    if (lane == 0) flag[WAVES - 1] = 1;
  }
  if (fq >= 0 && wave == 0) {
    unsigned rb[8], mlofs = (lane & 31) * RECB, gofs = lane * 16;
#pragma unroll
    for (int i = 0; i < 8; ++i) rb[i] = (hk * GQ + fq) * (NSPLIT * RECB) + i * (4 * RECB);
    asm volatile("" : "+s"(rb[0]), "+s"(rb[1]), "+s"(rb[2]), "+s"(rb[3]), "+s"(rb[4]), "+s"(rb[5]), "+s"(rb[6]), "+s"(rb[7]), "+v"(mlofs), "+v"(gofs));
    unsigned* const optr = reinterpret_cast<unsigned*>(out + (hk * GQ + fq) * D + 64 * (lane >> 5) + 4 * col + 2 * (g & 1));
    float o0, o1, inv;
    {
      u32x4 wl; unsigned wo;
      ld_rec<1040>(wl, mlofs, srec, rb[0]);
      asm volatile("global_load_dword %0, %1, off" : "=v"(wo) : "v"(optr) : "memory");
      WAIT2(0, wl, wo);
    }
    while (flag[WAVES - 1] == 0) __builtin_amdgcn_s_sleep(1);
    { const unsigned go = now32() + GDELAY; while ((int)(now32() - go) < 0) {} }
    for (;;) {
      u32x4 ML, G[NSPLIT];
      ld_rec<1024>(ML, mlofs, srec, rb[0]);
      Gather<0>::issue(G, gofs, srec, rb);
      WAIT1(NSPLIT, ML);
      const float mi = __uint_as_float(ML[0]), li = __uint_as_float(ML[2]);
      float Mx = mi;
      Mx = fmaxf(Mx, dpp<XOR1>(Mx)); Mx = fmaxf(Mx, dpp<XOR2>(Mx)); Mx = fmaxf(Mx, dpp<HMIRROR>(Mx)); Mx = fmaxf(Mx, dpp<MIRROR>(Mx)); Mx = x16_max(Mx);
      const float fi = __builtin_amdgcn_exp2f(mi - Mx);
      float Ls = fi * li;
      Ls += dpp<XOR1>(Ls); Ls += dpp<XOR2>(Ls); Ls += dpp<HMIRROR>(Ls); Ls += dpp<MIRROR>(Ls); Ls = x16_sum(Ls);
      inv = 1.f / Ls;
      PIN(inv);
      unsigned bad = (ML[1] ^ tagf) | (ML[3] ^ tagf);
      o0 = 0.f; o1 = 0.f;
      Gather<0>::sum(G, fi, tagf, bad, o0, o1);
      if (__all(bad == 0) || now32() - (unsigned)tstart > GIVE_UP) break;
      while (now32() - (unsigned)tstart <= GIVE_UP) {
        ld_rec<1024>(ML, mlofs, srec, rb[0]);
        WAIT1(0, ML);
        if (__all(ML[1] == tagf && ML[3] == tagf)) break;
      }
    }
    *optr = pk_bf16(o0 * inv, o1 * inv);
    if (lane == 0) __hip_atomic_store((gu32*)(ws + hk * 128 + fq * 4), tagf, __ATOMIC_RELAXED, __HIP_MEMORY_SCOPE_AGENT);
  }
}

}

extern "C" void kernel_launch(void* const* d_in, const int* in_sizes, int n_in, void* d_out, int out_size, void* d_ws,
                              size_t ws_size, hipStream_t stream) {
  attn_decode<<<HKV * NSPLIT, NTHR, 0, stream>>>((const unsigned short*)d_in[0], (const int*)d_in[1], (const int*)d_in[2],
                                                 (const unsigned short*)d_in[3], (const unsigned short*)d_in[4],
                                                 (const unsigned short*)d_in[5], (const unsigned short*)d_in[6],
                                                 (unsigned char*)d_ws, (unsigned short*)d_out);
}
```
